```python
import torch

def _softmax(x, axis=-1):
    return torch.softmax(x, dim=axis)
def _gelu(x):
    return torch.nn.functional.gelu(x, approximate="tanh")
def _silu(x):
    return torch.nn.functional.silu(x)
def _sigmoid(x):
    return torch.sigmoid(x)
def _layernorm(x, axis=-1, eps=1e-5):
    m = x.mean(dim=axis, keepdim=True)
    v = x.var(dim=axis, keepdim=True, unbiased=False)
    return (x - m) / torch.sqrt(v + eps)
def _scatter_add(tgt, idx, src, axis=0):
    return tgt.clone().index_add_(axis, idx, src)
def _segment_sum(src, seg, n):
    out = torch.zeros((n,) + src.shape[1:], dtype=src.dtype)
    return out.index_add_(0, seg, src)
def _pad(x, pw):
    flat = tuple(v for pair in reversed(pw) for v in pair)
    return torch.nn.functional.pad(x, flat)

kernel_name = 'standalone__paged_block_table_kv_radixattention__decode'

def setup_inputs():

    _w = (lambda a: (lambda t: t.to(torch.bfloat16) if t.is_floating_point() else t)(torch.as_tensor(a)))
    B, H, Hkv, Dh = 1, 32, 4, 128
    Lk = 65536
    bs = 16
    nb = max(2, Lk // bs)
    P = max(1, nb // 2)
    n_phys = P + B * (nb - P)
    shared = torch.arange(P, dtype=torch.int64)
    priv = P + torch.arange(B * (nb - P), dtype=torch.int64).reshape(B, nb - P)
    bt = torch.cat([torch.tile(shared[None, :], (B, 1)), priv], dim=1)
    ctx = nb * bs - (torch.arange(1, B + 1, dtype=torch.int64) * 2654435761) % max(1, (nb * bs - P * bs) // 2)
    return {
        "q": _w((lambda *s: torch.randn(*s, dtype=torch.bfloat16) * 0.8)(B, H, Dh)),
        "k_cache": _w((lambda *s: torch.randn(*s, dtype=torch.bfloat16) * 0.8)(n_phys, bs, Hkv, Dh)),
        "v_cache": _w((lambda *s: torch.randn(*s, dtype=torch.bfloat16) * 0.05)(n_phys, bs, Hkv, Dh)),
        "block_table": _w(bt.to(torch.int64)),
        "ctx_lens": _w(torch.as_tensor(ctx).to(torch.int64)),
    }

def reference(q, k_cache, v_cache, block_table, ctx_lens):
    B, H, Dh = q.shape
    n_phys, bs, Hkv, _ = k_cache.shape
    nb = block_table.shape[1]
    Lk = nb * bs
    rep = H // Hkv
    k = k_cache[block_table].reshape(B, Lk, Hkv, Dh)
    v = v_cache[block_table].reshape(B, Lk, Hkv, Dh)
    k = (lambda x, n, axis: torch.repeat_interleave(x, n, dim=axis))(k, rep, axis=2)
    v = (lambda x, n, axis: torch.repeat_interleave(x, n, dim=axis))(v, rep, axis=2)
    scores = torch.einsum("bhd,bkhd->bhk", q, k) * (Dh ** -0.5)
    pos = torch.arange(Lk)
    valid = pos[None, :] < ctx_lens[:, None]
    scores = torch.where(valid[:, None, :], scores, -1e9)
    p = _softmax(scores, axis=-1)
    return torch.einsum("bhk,bkhd->bhd", p, v)
```

```cpp
#include <hip/hip_runtime.h>
#include <hip/hip_bf16.h>
#include <stdint.h>

namespace {
constexpr int H = 32, HKV = 4, D = 128, BS = 16, G = H / HKV;
constexpr int NB = 4096;
constexpr int NSPLIT = 64, NWAVE = 8, NTHR = NWAVE * 64;
constexpr int NGW = NSPLIT * NWAVE;
constexpr int NU = NB / 2 / NGW;
constexpr int PAGE_BYTES = BS * HKV * D * 2;
constexpr int TOK_BYTES = HKV * D * 2;
#define KV_AUX 2

#define FENCE() do { asm volatile("" ::: "memory"); __builtin_amdgcn_sched_barrier(0); } while (0)
using bf16x8 = __attribute__((ext_vector_type(8))) __bf16;
using bf16x2 = __attribute__((ext_vector_type(2))) __bf16;
using f32x4 = __attribute__((ext_vector_type(4))) float;
using u32x4 = __attribute__((ext_vector_type(4))) unsigned;
using rsrc_t = __amdgpu_buffer_rsrc_t;

__device__ __forceinline__ float xg_max(float x) {
  auto s = __builtin_amdgcn_permlane16_swap(__float_as_uint(x), __float_as_uint(x), false, false);
  x = fmaxf(__uint_as_float(s[0]), __uint_as_float(s[1]));
  auto t = __builtin_amdgcn_permlane32_swap(__float_as_uint(x), __float_as_uint(x), false, false);
  return fmaxf(__uint_as_float(t[0]), __uint_as_float(t[1]));
}
__device__ __forceinline__ float xg_sum(float x) {
  auto s = __builtin_amdgcn_permlane16_swap(__float_as_uint(x), __float_as_uint(x), false, false);
  x = __uint_as_float(s[0]) + __uint_as_float(s[1]);
  auto t = __builtin_amdgcn_permlane32_swap(__float_as_uint(x), __float_as_uint(x), false, false);
  return __uint_as_float(t[0]) + __uint_as_float(t[1]);
}
__device__ __forceinline__ unsigned pk_bf16(float lo, float hi) {
  bf16x2 v = {(__bf16)lo, (__bf16)hi};
  return __builtin_bit_cast(unsigned, v);
}
__device__ __forceinline__ f32x4 mfma16(u32x4 a, u32x4 b, f32x4 c) {
  return __builtin_amdgcn_mfma_f32_16x16x32_bf16(__builtin_bit_cast(bf16x8, a), __builtin_bit_cast(bf16x8, b), c, 0, 0, 0);
}

constexpr int REC = D + 4;

__global__ __launch_bounds__(NTHR, 2) void attn_split(const uint16_t* __restrict__ q, const uint16_t* __restrict__ kc,
                                                      const uint16_t* __restrict__ vc, const int* __restrict__ bt,
                                                      const int* __restrict__ lens, float* __restrict__ po,
                                                      float* __restrict__ pml) {
  __shared__ __attribute__((aligned(16))) float lds[NWAVE * G * REC];
  const int tid = threadIdx.x, lane = tid & 63;
  const int wave = __builtin_amdgcn_readfirstlane(tid >> 6);
  const int g = lane >> 4, c = lane & 15;
  const int kvh = blockIdx.x & (HKV - 1), split = blockIdx.x >> 2;
  const int gw = wave * NSPLIT + split;

  const int len_raw = lens[0];
  int2 pg[NU];
#pragma unroll
  for (int i = 0; i < NU; ++i) pg[i] = *reinterpret_cast<const int2*>(bt + 2 * (gw + i * NGW));

  u32x4 qf[4];
  {
    rsrc_t qr = __builtin_amdgcn_make_buffer_rsrc((void*)(q + kvh * G * D), 0, G * D * 2, 0x00020000);
#pragma unroll
    for (int s = 0; s < 4; ++s) qf[s] = __builtin_amdgcn_raw_buffer_load_b128(qr, c * 256 + g * 16 + s * 64, 0, 0);
  }
  FENCE();
  const bool uni = len_raw <= 0;
  const int len = uni ? NB * BS : min(len_raw, NB * BS);
  const int koff = c * TOK_BYTES + g * 16;
  const int voff = g * 4 * TOK_BYTES + c * 16;

  u32x4 kb[2][8], vb[2][8];
  auto issue_k = [&](int i, u32x4 (&dst)[8]) {
    const int tok0 = (gw + i * NGW) * 32;
#pragma unroll
    for (int t = 0; t < 2; ++t) {
      const int nv = uni ? 0 : max(0, len - tok0 - t * BS);
      const int p = t ? pg[i].y : pg[i].x;
      rsrc_t r = __builtin_amdgcn_make_buffer_rsrc((void*)((const char*)kc + (size_t)p * PAGE_BYTES + kvh * 256), 0, nv * TOK_BYTES, 0x00020000);
#pragma unroll
      for (int s = 0; s < 4; ++s) dst[t * 4 + s] = __builtin_amdgcn_raw_buffer_load_b128(r, koff + s * 64, 0, KV_AUX);
    }
    FENCE();
  };
  auto issue_v = [&](int i, u32x4 (&dst)[8]) {
    const int tok0 = (gw + i * NGW) * 32;
#pragma unroll
    for (int t = 0; t < 2; ++t) {
      const int nv = max(0, len - tok0 - t * BS);
      const int p = t ? pg[i].y : pg[i].x;
      rsrc_t r = __builtin_amdgcn_make_buffer_rsrc((void*)((const char*)vc + (size_t)p * PAGE_BYTES + kvh * 256), 0, nv * TOK_BYTES, 0x00020000);
#pragma unroll
      for (int j = 0; j < 4; ++j) dst[t * 4 + j] = __builtin_amdgcn_raw_buffer_load_b128(r, voff + j * TOK_BYTES, 0, KV_AUX);
    }
    FENCE();
  };

  constexpr float C2 = 0.088388347648318440f * 1.4426950408889634f;
  float m = -INFINITY, l = 0.f;
  f32x4 acc[8];
#pragma unroll
  for (int i = 0; i < 8; ++i) acc[i] = f32x4{0.f, 0.f, 0.f, 0.f};

  issue_k(0, kb[0]); issue_v(0, vb[0]);
  if (NU > 1) { issue_k(1, kb[1]); issue_v(1, vb[1]); }

#pragma unroll
  for (int i = 0; i < NU; ++i) {
    const int tok0 = (gw + i * NGW) * 32;
    const bool live = tok0 < len;
    u32x4 pf;
    if (live) {
      f32x4 s0 = {0.f, 0.f, 0.f, 0.f}, s1 = {0.f, 0.f, 0.f, 0.f};
#pragma unroll
      for (int s = 0; s < 4; ++s) { s0 = mfma16(kb[i & 1][s], qf[s], s0); s1 = mfma16(kb[i & 1][4 + s], qf[s], s1); }
      float x[8];
#pragma unroll
      for (int r = 0; r < 4; ++r) { x[r] = s0[r] * C2; x[4 + r] = s1[r] * C2; }
      if (tok0 + 32 > len) {
#pragma unroll
        for (int j = 0; j < 8; ++j) x[j] = (tok0 + (j >> 2) * 16 + 4 * g + (j & 3) < len) ? x[j] : -INFINITY;
      }
      float mx = fmaxf(fmaxf(fmaxf(x[0], x[1]), fmaxf(x[2], x[3])), fmaxf(fmaxf(x[4], x[5]), fmaxf(x[6], x[7])));
      mx = xg_max(mx);
      const float mn = fmaxf(m, mx);
      const float alpha = __builtin_amdgcn_exp2f(m - mn);
      m = mn;
      float ps = 0.f;
#pragma unroll
      for (int j = 0; j < 8; ++j) { x[j] = __builtin_amdgcn_exp2f(x[j] - mn); ps += x[j]; }
      l = l * alpha + ps;
      pf = u32x4{pk_bf16(x[0], x[1]), pk_bf16(x[2], x[3]), pk_bf16(x[4], x[5]), pk_bf16(x[6], x[7])};
      if (i > 0) {
#pragma unroll
        for (int k = 0; k < 8; ++k) acc[k] *= alpha;
      }
    }
    if (i + 2 < NU) issue_k(i + 2, kb[i & 1]);
    if (live) {
#pragma unroll
      for (int k = 0; k < 8; ++k) {
        const unsigned sel = (k & 1) ? 0x07060302u : 0x05040100u;
        u32x4 a;
#pragma unroll
        for (int qd = 0; qd < 4; ++qd) a[qd] = __builtin_amdgcn_perm(vb[i & 1][2 * qd + 1][k >> 1], vb[i & 1][2 * qd][k >> 1], sel);
        acc[k] = mfma16(a, pf, acc[k]);
      }
    }
    if (i + 2 < NU) issue_v(i + 2, vb[i & 1]);
  }

  l = xg_sum(l);
  if (c < G) {
    float* rec = lds + (wave * G + c) * REC;
#pragma unroll
    for (int r = 0; r < 4; ++r) {
      *reinterpret_cast<f32x4*>(rec + 32 * g + 8 * r) = f32x4{acc[0][r], acc[1][r], acc[2][r], acc[3][r]};
      *reinterpret_cast<f32x4*>(rec + 32 * g + 8 * r + 4) = f32x4{acc[4][r], acc[5][r], acc[6][r], acc[7][r]};
    }
    if (g == 0) { rec[D] = m; rec[D + 1] = l; }
  }
  __syncthreads();
  if (tid < G * 32) {
    const int h = tid >> 5, d4 = tid & 31;
    float mw[NWAVE], M = -INFINITY;
#pragma unroll
    for (int w = 0; w < NWAVE; ++w) { mw[w] = lds[(w * G + h) * REC + D]; M = fmaxf(M, mw[w]); }
    float L = 0.f;
    f32x4 o = {0.f, 0.f, 0.f, 0.f};
#pragma unroll
    for (int w = 0; w < NWAVE; ++w) {
      const float* rec = lds + (w * G + h) * REC;
      const float wt = (mw[w] == -INFINITY) ? 0.f : __builtin_amdgcn_exp2f(mw[w] - M);
      L += wt * rec[D + 1];
      o += wt * *reinterpret_cast<const f32x4*>(rec + 4 * d4);
    }
    const int slot = (kvh * G + h) * NSPLIT + split;
    *reinterpret_cast<f32x4*>(po + (size_t)slot * D + 4 * d4) = o;
    if (d4 == 0) *reinterpret_cast<float2*>(pml + 2 * slot) = make_float2(M, L);
  }
}

__global__ __launch_bounds__(64) void attn_combine(const float* __restrict__ po, const float* __restrict__ pml, uint16_t* __restrict__ out) {
  const int h = blockIdx.x >> 2, dq = blockIdx.x & 3, lane = threadIdx.x, sg = lane >> 3, d4 = lane & 7;
  float2 ml[8];
  f32x4 v[8];
#pragma unroll
  for (int k = 0; k < 8; ++k) {
    const int slot = h * NSPLIT + sg + 8 * k;
    ml[k] = *reinterpret_cast<const float2*>(pml + 2 * slot);
    v[k] = *reinterpret_cast<const f32x4*>(po + (size_t)slot * D + dq * 32 + d4 * 4);
  }
  float M = -INFINITY;
#pragma unroll
  for (int k = 0; k < 8; ++k) M = fmaxf(M, ml[k].x);
  M = fmaxf(M, __shfl_xor(M, 8));
  M = xg_max(M);
  float L = 0.f;
  f32x4 o = {0.f, 0.f, 0.f, 0.f};
#pragma unroll
  for (int k = 0; k < 8; ++k) {
    const float wt = (ml[k].x == -INFINITY) ? 0.f : __builtin_amdgcn_exp2f(ml[k].x - M);
    L += wt * ml[k].y;
    o += wt * v[k];
  }
  L += __shfl_xor(L, 8); L = xg_sum(L);
#pragma unroll
  for (int e = 0; e < 4; ++e) { o[e] += __shfl_xor(o[e], 8); o[e] = xg_sum(o[e]); }
  if (sg == 0) {
    const float inv = 1.f / L;
    uint2 r = make_uint2(pk_bf16(o[0] * inv, o[1] * inv), pk_bf16(o[2] * inv, o[3] * inv));
    *reinterpret_cast<uint2*>(out + h * D + dq * 32 + d4 * 4) = r;
  }
}
}

extern "C" void kernel_launch(void* const* d_in, const int* in_sizes, int n_in, void* d_out, int out_size, void* d_ws,
                              size_t ws_size, hipStream_t stream) {
  const uint16_t* q = (const uint16_t*)d_in[0];
  const uint16_t* kc = (const uint16_t*)d_in[1];
  const uint16_t* vc = (const uint16_t*)d_in[2];
  const int* bt = (const int*)d_in[3];
  const int* lens = (const int*)d_in[4];
  float* po = (float*)d_ws;
  float* pml = po + (size_t)H * NSPLIT * D;
  attn_split<<<dim3(NSPLIT * HKV), dim3(NTHR), 0, stream>>>(q, kc, vc, bt, lens, po, pml);
  attn_combine<<<dim3(H * 4), dim3(64), 0, stream>>>(po, pml, (uint16_t*)d_out);
}
```

```cpp
#include <hip/hip_runtime.h>
#include <hip/hip_bf16.h>
#include <stdint.h>

#define ST(i) do {} while (0)
#define POLL_T1 32
#define POLL_S1 16
namespace {
constexpr int H = 32, HKV = 4, D = 128, BS = 16;
constexpr int NB = 4096;
constexpr int NWG = 256, NWAVE = 16, NTHR = NWAVE * 64, NWORK = 12;
constexpr int PAGE_BYTES = BS * HKV * D * 2;
static_assert(NB * 2 / NWG == 32, "at most 32 units per workgroup");
constexpr int LDS_V = 0;
constexpr int LDS_S = 131072;
constexpr int LDS_Q = LDS_S + 16384;
constexpr int LDS_FL = LDS_Q + 8192;
constexpr int LDS_BT = LDS_FL + 128;
constexpr int LDS_TAG = LDS_BT + 128;
constexpr int LDS_BYTES = LDS_TAG + 64;
constexpr int F_CNT = 0, F_DONE = 16, F_NEXT = 24, F_PUB = 25, F_FIN = 26, F_GO = 27;
constexpr int INBOX_BYTES = NWG * 64;
constexpr int WS_EPOCH = NWG * INBOX_BYTES;
constexpr int WS_HINT = WS_EPOCH + 4096;
constexpr unsigned TAG_MASK = 0xf00u, DATA_MASK = 0xfffff000u;
constexpr unsigned SPIN_CAP = 1u << 15;

#define LAS __attribute__((address_space(3)))
using bf16x8 = __attribute__((ext_vector_type(8))) __bf16;
using bf16x2 = __attribute__((ext_vector_type(2))) __bf16;
using s16x4 = __attribute__((ext_vector_type(4))) short;
using f32x4 = __attribute__((ext_vector_type(4))) float;
using u32x4 = __attribute__((ext_vector_type(4))) unsigned;
using u32x2 = __attribute__((ext_vector_type(2))) unsigned;
using f32x2 = __attribute__((ext_vector_type(2))) float;

template <int CTRL> __device__ __forceinline__ float dpp(float x) {
  return __builtin_bit_cast(float, __builtin_amdgcn_mov_dpp(__builtin_bit_cast(int, x), CTRL, 0xf, 0xf, true));
}
constexpr int ROR4 = 0x124, ROR8 = 0x128;
__device__ __forceinline__ float x16_sum(float x) {
  auto s = __builtin_amdgcn_permlane16_swap(__float_as_uint(x), __float_as_uint(x), false, false);
  return __uint_as_float(s[0]) + __uint_as_float(s[1]);
}
__device__ __forceinline__ float x16_max(float x) {
  auto s = __builtin_amdgcn_permlane16_swap(__float_as_uint(x), __float_as_uint(x), false, false);
  return fmaxf(__uint_as_float(s[0]), __uint_as_float(s[1]));
}
__device__ __forceinline__ float xg_max(float x) {
  x = x16_max(x);
  auto t = __builtin_amdgcn_permlane32_swap(__float_as_uint(x), __float_as_uint(x), false, false);
  return fmaxf(__uint_as_float(t[0]), __uint_as_float(t[1]));
}
__device__ __forceinline__ float xg_sum(float x) {
  x = x16_sum(x);
  auto t = __builtin_amdgcn_permlane32_swap(__float_as_uint(x), __float_as_uint(x), false, false);
  return __uint_as_float(t[0]) + __uint_as_float(t[1]);
}
__device__ __forceinline__ unsigned pk_bf16(float lo, float hi) {
  bf16x2 v = {(__bf16)lo, (__bf16)hi};
  return __builtin_bit_cast(unsigned, v);
}
__device__ __forceinline__ f32x4 mfma32(u32x4 a, u32x4 b, f32x4 c) {
  return __builtin_amdgcn_mfma_f32_16x16x32_bf16(__builtin_bit_cast(bf16x8, a), __builtin_bit_cast(bf16x8, b), c, 0, 0, 0);
}
__device__ __forceinline__ u32x4 make_srd(const void* base, unsigned nbytes) {
  const uint64_t a = (uint64_t)base;
  return u32x4{(unsigned)a, (unsigned)(a >> 32), nbytes, 0x00020000u};
}
__device__ __forceinline__ void load_k(u32x4 (&d)[8], int voff, u32x4 srd) {
  asm volatile("buffer_load_dwordx4 %0, %8, %9, 0 offen nt\n\t"
               "buffer_load_dwordx4 %1, %8, %9, 0 offen offset:128 nt\n\t"
               "buffer_load_dwordx4 %2, %8, %9, 0 offen offset:256 nt\n\t"
               "buffer_load_dwordx4 %3, %8, %9, 0 offen offset:384 nt\n\t"
               "buffer_load_dwordx4 %4, %8, %9, 0 offen offset:512 nt\n\t"
               "buffer_load_dwordx4 %5, %8, %9, 0 offen offset:640 nt\n\t"
               "buffer_load_dwordx4 %6, %8, %9, 0 offen offset:768 nt\n\t"
               "buffer_load_dwordx4 %7, %8, %9, 0 offen offset:896 nt"
               : "=&v"(d[0]), "=&v"(d[1]), "=&v"(d[2]), "=&v"(d[3]), "=&v"(d[4]), "=&v"(d[5]), "=&v"(d[6]), "=&v"(d[7])
               : "v"(voff), "s"(srd));
  __builtin_amdgcn_sched_barrier(0);
}
__device__ __forceinline__ void load_v(unsigned dst, const int (&vo)[4], u32x4 srd) {
  asm volatile("s_mov_b32 m0, %0\n\ts_nop 0\n\t"
               "buffer_load_dwordx4 %2, %6, 0 offen nt lds\n\t"
               "buffer_load_dwordx4 %3, %6, 0 offen offset:1024 nt lds\n\t"
               "buffer_load_dwordx4 %4, %6, 0 offen offset:2048 nt lds\n\t"
               "buffer_load_dwordx4 %5, %6, 0 offen offset:3072 nt lds\n\t"
               "s_mov_b32 m0, %1\n\ts_nop 0\n\t"
               "buffer_load_dwordx4 %2, %6, %7 offen nt lds\n\t"
               "buffer_load_dwordx4 %3, %6, %7 offen offset:1024 nt lds\n\t"
               "buffer_load_dwordx4 %4, %6, %7 offen offset:2048 nt lds\n\t"
               "buffer_load_dwordx4 %5, %6, %7 offen offset:3072 nt lds"
               :: "s"(dst), "s"(dst + 4096u), "v"(vo[0]), "v"(vo[1]), "v"(vo[2]), "v"(vo[3]), "s"(srd), "s"(4096)
               : "m0", "memory");
  __builtin_amdgcn_sched_barrier(0);
}
__device__ __forceinline__ unsigned hint_of(u32x4 t) { return 0x5a000000u | (t[0] >> 8) | (t[1] >> 4) | t[2] | (t[3] << 4); }
template <int N> __device__ __forceinline__ void wait_vm() { asm volatile("s_waitcnt vmcnt(%0)" :: "i"(N) : "memory"); }
__device__ __forceinline__ void wg_barrier() { asm volatile("s_waitcnt lgkmcnt(0)\n\ts_barrier" ::: "memory"); }
__device__ __forceinline__ int flag_get(LAS char* lds, int i) {
  return __builtin_amdgcn_readfirstlane(__hip_atomic_load((LAS int*)(lds + LDS_FL) + i, __ATOMIC_RELAXED, __HIP_MEMORY_SCOPE_WORKGROUP));
}
__device__ __forceinline__ void flag_add(LAS char* lds, int i, int) {
  asm volatile("s_waitcnt lgkmcnt(0)\n\t"
               "s_mov_b64 exec, 1\n\t"
               "ds_add_u32 %0, %1\n\t"
               "s_mov_b64 exec, -1"
               :: "v"((int)(uintptr_t)(lds + LDS_FL) + i * 4), "v"(1) : "memory");
}
__device__ __forceinline__ int flag_inc2(LAS char* lds, int a, int b) {
  int r;
  asm volatile("s_mov_b64 exec, 1\n\t"
               "ds_add_u32 %1, %3\n\t"
               "ds_add_rtn_u32 %0, %2, %3\n\t"
               "s_mov_b64 exec, -1\n\t"
               "s_waitcnt lgkmcnt(0)"
               : "=&v"(r) : "v"((int)(uintptr_t)(lds + LDS_FL) + a * 4), "v"((int)(uintptr_t)(lds + LDS_FL) + b * 4), "v"(1) : "memory");
  return __builtin_amdgcn_readfirstlane(r);
}
template <int SLEEP>
__device__ __forceinline__ void flag_wait(LAS char* lds, int i, int v) {
  for (unsigned spin = 0; flag_get(lds, i) < v && spin < (1u << 18); ++spin) __builtin_amdgcn_s_sleep(SLEEP);
  asm volatile("" ::: "memory");
}
#define LEAD 8
#define HINT_THR 232
#define PRETOUCH 0
#define LOOK 0
#define EARLY 8
#define ISSW 3

__global__ __launch_bounds__(NTHR, 4) void attn_decode(const uint16_t* __restrict__ q, const uint16_t* __restrict__ kc,
                                                       const uint16_t* __restrict__ vc, const int* __restrict__ bt,
                                                       const int* __restrict__ lens, char* __restrict__ ws,
                                                       uint16_t* __restrict__ out) {
  __shared__ __attribute__((aligned(1024))) char smem[LDS_BYTES];
  LAS char* lds = (LAS char*)smem;
  ST(0);
  const int tid = threadIdx.x, lane = tid & 63;
  const int wave = __builtin_amdgcn_readfirstlane(tid >> 6);
  const int g = lane >> 4, c = lane & 15;
  const bool lo = c < 8;
  const int wg = blockIdx.x;

  asm volatile("" ::"s"(ws), "s"(vc), "s"(kc), "s"(out), "s"(q));
  u32x4 tag;
#define LEN_DERIVED() \
  const bool uni = len_raw <= 0;                 \
  const int len = uni ? NB * BS : min(len_raw, NB * BS); \
  const int nW = (((len + 7) >> 3) - wg + NWG - 1) / NWG;           \
  const int nG = (nW + 3) >> 2

  if (wave < 4) {
    __builtin_amdgcn_s_setprio(3);
    int len_raw;
    {
      const int qoff = ((8 * wave + (c & 7)) * D + 32 * ((c >> 3) ^ (wave & 1)) + 8 * g) * 2;
      u32x4 q0, q1;
      int btv, touch;
      asm volatile("global_load_dwordx4 %0, %5, %6\n\t"
                   "global_load_dwordx4 %1, %5, %6 offset:128\n\t"
                   "global_load_dword %2, %7, %8\n\t"
                   "global_load_dwordx4 %3, %9, %10\n\t"
                   "global_load_dword %4, %11, %12 sc1"
                   : "=&v"(q0), "=&v"(q1), "=&v"(btv), "=&v"(tag), "=&v"(touch)
                   : "v"(qoff), "s"(q), "v"(((wg >> 1) + 128 * (wave * 8 + (lane & 7))) * 4), "s"(bt), "v"((lane & 3) * 16), "s"(ws + WS_EPOCH),
                     "v"(PRETOUCH ? tid * 64 : 0), "s"(ws + (size_t)wg * INBOX_BYTES));
      asm volatile("s_load_dword %0, %1, 0x0\n\ts_waitcnt lgkmcnt(0)" : "=&s"(len_raw) : "s"(lens));
      asm volatile("s_waitcnt vmcnt(1)" : "+v"(q0), "+v"(q1), "+v"(btv), "+v"(tag));
      *(LAS u32x4*)(lds + LDS_Q + wave * 2048 + lane * 16) = q0;
      *(LAS u32x4*)(lds + LDS_Q + wave * 2048 + 1024 + lane * 16) = q1;
      *(LAS int*)(lds + LDS_BT + (wave * 8 + (lane & 7)) * 4) = btv;
#pragma unroll
      for (int e = 0; e < 4; ++e) tag[e] = (tag[e] + 0x100u) & TAG_MASK;
      *(LAS u32x4*)(lds + LDS_TAG + (lane & 3) * 16) = tag;
      *((LAS int*)(lds + LDS_FL) + (lane & 31)) = (lane & 31) == F_NEXT ? NWORK : 0;
      ST(1);
      wg_barrier();
      asm volatile("s_waitcnt vmcnt(0)" : "+v"(touch));
    }
    LEN_DERIVED();
    float M[2] = {-INFINITY, -INFINITY}, l[2] = {0.f, 0.f};
    f32x4 acc[2][2];
#pragma unroll
    for (int s = 0; s < 2; ++s)
#pragma unroll
      for (int hf = 0; hf < 2; ++hf) acc[s][hf] = f32x4{0.f, 0.f, 0.f, 0.f};
    int trb[2];
#pragma unroll
    for (int s = 0; s < 2; ++s)
      trb[s] = LDS_V + g * 8192 + (c >> 2) * 1024 + (2 * ((2 * wave + s) ^ ((c >> 2) | ((g & 1) << 2))) + ((c >> 1) & 1)) * 16 + (c & 1) * 8;
    unsigned low[2][4];
    float inv[2] = {0.f, 0.f};
#pragma unroll
    for (int hf = 0; hf < 2; ++hf)
#pragma unroll
      for (int e = 0; e < 4; ++e) low[hf][e] = tag[e] | ((0xff800000u >> (8 * e)) & 0xffu);
    u32x4 pn[2];
    float aln[2];
    auto stage_a = [&](int G) {
      flag_wait<1>(lds, F_CNT + 2 * G, min(4, nW - 4 * G));
      const bool live = 4 * G + g < nW;
      f32x4 s0[2], s1[2];
#pragma unroll
      for (int hf = 0; hf < 2; ++hf) {
        const int so = LDS_S + ((G & 3) * 4 + g) * 1024 + hf * 512 + c * 16;
        s0[hf] = *(const LAS f32x4*)(lds + so); s1[hf] = *(const LAS f32x4*)(lds + so + 256);
      }
#pragma unroll
      for (int hf = 0; hf < 2; ++hf) {
        float x[8];
#pragma unroll
        for (int e = 0; e < 4; ++e) { x[e] = live ? s0[hf][e] : -INFINITY; x[4 + e] = live ? s1[hf][e] : -INFINITY; }
        const float mx = fmaxf(fmaxf(fmaxf(x[0], x[1]), fmaxf(x[2], x[3])), fmaxf(fmaxf(x[4], x[5]), fmaxf(x[6], x[7])));
        const float mn = fmaxf(M[hf], xg_max(mx));
        aln[hf] = __builtin_amdgcn_exp2f(M[hf] - mn);
        M[hf] = mn;
        float ps = 0.f;
#pragma unroll
        for (int e = 0; e < 8; ++e) { x[e] = __builtin_amdgcn_exp2f(x[e] - mn); ps += x[e]; }
        l[hf] = l[hf] * aln[hf] + ps;
#pragma unroll
        for (int e = 0; e < 4; ++e) pn[hf][e] = pk_bf16(x[2 * e], x[2 * e + 1]);
      }
      if (G == nG - 1) {
#pragma unroll
        for (int hf = 0; hf < 2; ++hf) {
          const float L = xg_sum(l[hf]);
          inv[hf] = __builtin_amdgcn_rcpf(L);
          const unsigned lse = __float_as_uint(M[hf] + __builtin_amdgcn_logf(L));
#pragma unroll
          for (int e = 0; e < 4; ++e) low[hf][e] = tag[e] | ((lse >> (8 * e)) & 0xffu);
        }
      }
    };
    if (nG > 0) stage_a(0);
    for (int G = 0; G < nG; ++G) {
      u32x4 pe[2], po[2];
#pragma unroll
      for (int hf = 0; hf < 2; ++hf) {
#pragma unroll
        for (int e = 0; e < 4; ++e) { pe[hf][e] = lo ? pn[hf][e] : 0u; po[hf][e] = lo ? 0u : pn[hf][e]; }
#pragma unroll
        for (int s = 0; s < 2; ++s) acc[s][hf] *= aln[hf];
      }
      if (G + 1 < nG) stage_a(G + 1);
      flag_wait<0>(lds, F_CNT + 2 * G + 1, G < 4 ? 4 : min(4, nW - 4 * G));
      if (G == 0) ST(6);
      if (G == 2) ST(7);
      if (G == 4) ST(8);
      if (G == nG - 1) ST(9);
#pragma unroll
      for (int s = 0; s < 2; ++s)
#pragma unroll
        for (int hd = 0; hd < 4; ++hd) {
          const int o = trb[s] + (G & 3) * 32768 + hd * 256;
          const u32x2 a0 = __builtin_bit_cast(u32x2, __builtin_amdgcn_ds_read_tr16_b64_v4i16((LAS s16x4*)(lds + o)));
          const u32x2 a1 = __builtin_bit_cast(u32x2, __builtin_amdgcn_ds_read_tr16_b64_v4i16((LAS s16x4*)(lds + o + 4096)));
          acc[s][hd >> 1] = mfma32(u32x4{a0[0], a0[1], a1[0], a1[1]}, (hd & 1) ? po[hd >> 1] : pe[hd >> 1], acc[s][hd >> 1]);
        }
      flag_add(lds, F_DONE + G, lane);
    }
    ST(11);
    u32x4 msg[2][2];
#pragma unroll
    for (int hf = 0; hf < 2; ++hf)
#pragma unroll
      for (int s = 0; s < 2; ++s)
#pragma unroll
        for (int e = 0; e < 4; ++e) msg[s][hf][e] = ((__float_as_uint(acc[s][hf][e] * inv[hf]) + 0x800u) & DATA_MASK) | low[hf][e];
    const int moff = ((c >> 2) * 32 + 8 * wave + g) * INBOX_BYTES + wg * 64 + (c & 3) * 16;
    asm volatile("global_store_dwordx4 %0, %4, %8 sc1\n\t"
                 "global_store_dwordx4 %1, %5, %8 sc1\n\t"
                 "global_store_dwordx4 %2, %6, %8 sc1\n\t"
                 "global_store_dwordx4 %3, %7, %8 sc1\n\t"
                 "s_nop 1"
                 :: "v"(moff), "v"(moff + 128 * INBOX_BYTES), "v"(moff + 4 * INBOX_BYTES), "v"(moff + 132 * INBOX_BYTES),
                    "v"(msg[0][0]), "v"(msg[0][1]), "v"(msg[1][0]), "v"(msg[1][1]), "s"(ws) : "memory");
    flag_add(lds, F_PUB, lane);
    ST(13);
  } else {
    int len_raw, pg0;
    asm volatile("s_load_dword %0, %2, 0x0\n\t"
                 "s_load_dword %1, %3, 0x0\n\t"
                 "s_waitcnt lgkmcnt(0)"
                 : "=&s"(len_raw), "=&s"(pg0) : "s"(lens), "s"(bt + (wg >> 1) + 128 * (wave - 4)));
    ST(1);
    LEN_DERIVED();
    const int lenk = uni ? 0 : len;
    const int koff = (c >> 1) * 1024 + (c & 1) * 64 + g * 16;
    const char* kbase = (const char*)kc + (wg & 1) * 8192;
    const char* vbase = (const char*)vc + (wg & 1) * 8192;
    const unsigned vlds = (unsigned)(uintptr_t)(lds + LDS_V);
    const int first = min(nW, LEAD), nlate = nW - first;
    const int nV = nW < 16 ? 4 * nG : nW;
    const int ntask = nW + nV;
    u32x4 qf[8];
    constexpr float C2 = 0.088388347648318440f * 1.4426950408889634f;
    auto scores = [&](u32x4 (&kb)[8], int j) {
      asm volatile("s_waitcnt vmcnt(0)"
                   : "+v"(kb[0]), "+v"(kb[1]), "+v"(kb[2]), "+v"(kb[3]), "+v"(kb[4]), "+v"(kb[5]), "+v"(kb[6]), "+v"(kb[7])::"memory");
      const int tok0 = (wg + j * NWG) * 8;
      f32x4 d[4];
#pragma unroll
      for (int hd = 0; hd < 4; ++hd) {
        d[hd] = mfma32(kb[2 * hd], qf[2 * hd], f32x4{0.f, 0.f, 0.f, 0.f});
        d[hd] = mfma32(kb[2 * hd + 1], qf[2 * hd + 1], d[hd]);
      }
#pragma unroll
      for (int hf = 0; hf < 2; ++hf) {
        f32x2 sc;
#pragma unroll
        for (int a = 0; a < 2; ++a) {
          const float u = lo ? d[2 * hf][2 * a] : d[2 * hf + 1][2 * a];
          const float w = lo ? d[2 * hf + 1][2 * a + 1] : d[2 * hf][2 * a + 1];
          const float x = (u + dpp<ROR8>(w)) * C2;
          sc[a] = (tok0 + 2 * g + a < len) ? x : -INFINITY;
        }
        *(LAS f32x2*)(lds + LDS_S + (j & 15) * 1024 + hf * 512 + (g >> 1) * 256 + c * 16 + (g & 1) * 8) = sc;
      }
      flag_add(lds, F_CNT + 2 * (j >> 2), lane);
    };
    auto read_q = [&]() {
#pragma unroll
      for (int f = 0; f < 8; ++f) qf[f] = *(const LAS u32x4*)(lds + LDS_Q + f * 1024 + lane * 16);
    };
    int s = wave - 4;
    if (s < first && s < EARLY) {
      u32x4 kb[8];
      load_k(kb, koff, make_srd(kbase + (size_t)(unsigned)pg0 * PAGE_BYTES, max(lenk - (wg + s * NWG) * 8, 0) * 1024));
      wg_barrier();
      const int snext = flag_inc2(lds, F_FIN, F_NEXT);
      read_q();
      scores(kb, s);
      s = snext;
    } else {
      wg_barrier();
      read_q();
    }
    ST(2);
    while (s < ntask) {
      const int t = s - first;
      const bool isk = t < 0 || (t < 2 * nlate && (t & 1));
      const int j = t < 0 ? s : (t < 2 * nlate ? (t >> 1) + ((t & 1) ? first : 0) : t - nlate);
      if (j >= 16) flag_wait<1>(lds, F_DONE + (j >> 2) - 4, 4);
      const unsigned pg = __builtin_amdgcn_readfirstlane(*(const LAS int*)(lds + LDS_BT + j * 4));
      const int tok0 = (wg + j * NWG) * 8;
      flag_wait<0>(lds, F_FIN, s - ISSW + 1);
      if (isk) {
        u32x4 kb[8];
        load_k(kb, koff, make_srd(kbase + (size_t)pg * PAGE_BYTES, max(lenk - tok0, 0) * 1024));
        s = flag_inc2(lds, F_FIN, F_NEXT);
        scores(kb, j);
      } else {
        if (j < nW) {
          int vo[4];
#pragma unroll
          for (int t4 = 0; t4 < 4; ++t4) vo[t4] = (lane ^ ((t4 | ((j & 1) << 2)) << 1)) * 16;
          load_v(vlds + (j & 15) * 8192, vo, make_srd(vbase + (size_t)pg * PAGE_BYTES, (len - tok0) * 1024));
        } else {
#pragma unroll
          for (int k = 0; k < 8; ++k) *(LAS u32x4*)(lds + LDS_V + j * 8192 + k * 1024 + lane * 16) = u32x4{0u, 0u, 0u, 0u};
        }
        s = flag_inc2(lds, F_FIN, F_NEXT);
        wait_vm<0>();
        flag_add(lds, F_CNT + 2 * (j >> 2) + 1, lane);
      }
    }
    ST(5);
    if (wave >= 8) {
      tag = *(const LAS u32x4*)(lds + LDS_TAG + (lane & 3) * 16);
      flag_wait<1>(lds, F_FIN, ntask - LOOK);
      ST(12);
      const int ct = tid - 512;
      const char* inbox = ws + (size_t)wg * INBOX_BYTES;
      u32x4 pc[2];
      unsigned long long act[2] = {~0ull, ~0ull};
      bool watch = false;
      for (unsigned spin = 0; spin < SPIN_CAP; ++spin) {
        asm volatile("s_mov_b64 exec, %4\n\t"
                     "global_load_dwordx4 %0, %2, %6 sc1\n\t"
                     "s_mov_b64 exec, %5\n\t"
                     "global_load_dwordx4 %1, %3, %6 sc1\n\t"
                     "s_mov_b64 exec, -1\n\t"
                     "s_waitcnt vmcnt(0)"
                     : "+v"(pc[0]), "+v"(pc[1]) : "v"(ct * 16), "v"(ct * 16 + 8192), "s"(act[0]), "s"(act[1]), "s"(inbox) : "memory");
        unsigned miss = 0u;
#pragma unroll
        for (int k = 0; k < 2; ++k) {
          unsigned x = 0;
#pragma unroll
          for (int e = 0; e < 4; ++e) x |= pc[k][e] ^ tag[e];
          miss |= (x & TAG_MASK) ? (1u << k) : 0u;
        }
        miss |= __builtin_amdgcn_mov_dpp(miss, 0xB1, 0xf, 0xf, true);
        miss |= __builtin_amdgcn_mov_dpp(miss, 0x4E, 0xf, 0xf, true);
        const unsigned long long m0 = __builtin_amdgcn_ballot_w64(miss & 1u), m1 = __builtin_amdgcn_ballot_w64(miss & 2u);
        const int nmiss = __builtin_popcountll(m0) + __builtin_popcountll(m1);
        if (nmiss == 0) break;
        if (spin == 0) {
          flag_wait<1>(lds, F_PUB, 4);
          act[0] = m0; act[1] = m1;
        } else if (watch) {
          act[0] &= m0; act[1] &= m1;
          if ((act[0] | act[1]) == 0ull) { act[0] = m0; act[1] = m1; watch = false; }
        } else if (nmiss > POLL_T1) {
          act[0] = (m0 & -m0) * 15ull; act[1] = (m1 & -m1) * 15ull; watch = true;
        } else {
          act[0] = m0; act[1] = m1;
        }
      }
      ST(14);
      float ls[2];
#pragma unroll
      for (int k = 0; k < 2; ++k)
        ls[k] = __uint_as_float(__builtin_amdgcn_perm(__builtin_amdgcn_perm(pc[k][3], pc[k][2], 0x0c0c0400u), __builtin_amdgcn_perm(pc[k][1], pc[k][0], 0x0c0c0400u), 0x05040100u));
      float Mx = fmaxf(ls[0], ls[1]);
      Mx = fmaxf(Mx, dpp<ROR4>(Mx)); Mx = fmaxf(Mx, dpp<ROR8>(Mx)); Mx = xg_max(Mx);
      const float Ms = (Mx == -INFINITY) ? 0.f : Mx;
      const float w0 = __builtin_amdgcn_exp2f(ls[0] - Ms), w1 = __builtin_amdgcn_exp2f(ls[1] - Ms);
      float v[5];
#pragma unroll
      for (int e = 0; e < 4; ++e) v[e] = w0 * __uint_as_float(pc[0][e] & DATA_MASK) + w1 * __uint_as_float(pc[1][e] & DATA_MASK);
      v[4] = w0 + w1;
#pragma unroll
      for (int e = 0; e < 5; ++e) { v[e] += dpp<ROR4>(v[e]); v[e] += dpp<ROR8>(v[e]); v[e] = xg_sum(v[e]); }
      if (lane < 4) {
        *(LAS f32x4*)(lds + LDS_S + ((wave - 8) * 4 + lane) * 32) = f32x4{v[0], v[1], v[2], v[3]};
        *(LAS f32x2*)(lds + LDS_S + ((wave - 8) * 4 + lane) * 32 + 16) = f32x2{v[4], Mx};
      }
      wg_barrier();
    }
  }
  if (wave == 8) {
    const int r = lane & 31;
    const f32x4 o = *(const LAS f32x4*)(lds + LDS_S + r * 32);
    const f32x2 wm = *(const LAS f32x2*)(lds + LDS_S + r * 32 + 16);
    float MM = wm[1];
    MM = fmaxf(MM, dpp<ROR4>(MM)); MM = fmaxf(MM, dpp<ROR8>(MM)); MM = x16_max(MM);
    const float wt = __builtin_amdgcn_exp2f(wm[1] - MM);
    float u[5] = {o[0] * wt, o[1] * wt, o[2] * wt, o[3] * wt, wm[0] * wt};
#pragma unroll
    for (int e = 0; e < 5; ++e) { u[e] += dpp<ROR4>(u[e]); u[e] += dpp<ROR8>(u[e]); u[e] = x16_sum(u[e]); }
    if (lane < 4) {
      const float inv = 1.f / u[4];
      *reinterpret_cast<uint2*>(out + (4 * (wg >> 5) + lane) * D + (wg & 31) * 4) = make_uint2(pk_bf16(u[0] * inv, u[1] * inv), pk_bf16(u[2] * inv, u[3] * inv));
      if (wg == 0) *reinterpret_cast<u32x4*>(ws + WS_EPOCH + lane * 16) = tag;
    }
  }
  ST(10);
}
}

extern "C" void kernel_launch(void* const* d_in, const int* in_sizes, int n_in, void* d_out, int out_size, void* d_ws,
                              size_t ws_size, hipStream_t stream) {
  attn_decode<<<dim3(NWG), dim3(NTHR), 0, stream>>>((const uint16_t*)d_in[0], (const uint16_t*)d_in[1], (const uint16_t*)d_in[2],
                                                    (const int*)d_in[3], (const int*)d_in[4], (char*)d_ws, (uint16_t*)d_out);
}
```

```cpp
#include <hip/hip_runtime.h>
#include <hip/hip_bf16.h>
#include <stdint.h>

#define ST(i) do {} while (0)
#define POLL_T1 32
#define POLL_S1 16
namespace {
constexpr int H = 32, HKV = 4, D = 128, BS = 16;
constexpr int NB = 4096;
constexpr int NWG = 256, NWAVE = 16, NTHR = NWAVE * 64, NWORK = 12;
constexpr int PAGE_BYTES = BS * HKV * D * 2;
static_assert(NB * 2 / NWG == 32, "at most 32 units per workgroup");
constexpr int LDS_V = 0;
constexpr int LDS_S = 131072;
constexpr int LDS_Q = LDS_S + 16384;
constexpr int LDS_FL = LDS_Q + 8192;
constexpr int LDS_BT = LDS_FL + 128;
constexpr int LDS_TAG = LDS_BT + 128;
constexpr int LDS_BYTES = LDS_TAG + 64;
constexpr int F_CNT = 0, F_DONE = 16, F_NEXT = 24, F_PUB = 25, F_FIN = 26, F_GO = 27;
constexpr int INBOX_BYTES = NWG * 64;
constexpr int WS_EPOCH = NWG * INBOX_BYTES;
constexpr int WS_HINT = WS_EPOCH + 4096;
constexpr unsigned TAG_MASK = 0xf00u, DATA_MASK = 0xfffff000u;
constexpr unsigned SPIN_CAP = 1u << 15;

#define LAS __attribute__((address_space(3)))
using bf16x8 = __attribute__((ext_vector_type(8))) __bf16;
using bf16x2 = __attribute__((ext_vector_type(2))) __bf16;
using s16x4 = __attribute__((ext_vector_type(4))) short;
using f32x4 = __attribute__((ext_vector_type(4))) float;
using u32x4 = __attribute__((ext_vector_type(4))) unsigned;
using u32x2 = __attribute__((ext_vector_type(2))) unsigned;
using f32x2 = __attribute__((ext_vector_type(2))) float;

template <int CTRL> __device__ __forceinline__ float dpp(float x) {
  return __builtin_bit_cast(float, __builtin_amdgcn_mov_dpp(__builtin_bit_cast(int, x), CTRL, 0xf, 0xf, true));
}
constexpr int ROR4 = 0x124, ROR8 = 0x128;
__device__ __forceinline__ float x16_sum(float x) {
  auto s = __builtin_amdgcn_permlane16_swap(__float_as_uint(x), __float_as_uint(x), false, false);
  return __uint_as_float(s[0]) + __uint_as_float(s[1]);
}
__device__ __forceinline__ float x16_max(float x) {
  auto s = __builtin_amdgcn_permlane16_swap(__float_as_uint(x), __float_as_uint(x), false, false);
  return fmaxf(__uint_as_float(s[0]), __uint_as_float(s[1]));
}
__device__ __forceinline__ float xg_max(float x) {
  x = x16_max(x);
  auto t = __builtin_amdgcn_permlane32_swap(__float_as_uint(x), __float_as_uint(x), false, false);
  return fmaxf(__uint_as_float(t[0]), __uint_as_float(t[1]));
}
__device__ __forceinline__ float xg_sum(float x) {
  x = x16_sum(x);
  auto t = __builtin_amdgcn_permlane32_swap(__float_as_uint(x), __float_as_uint(x), false, false);
  return __uint_as_float(t[0]) + __uint_as_float(t[1]);
}
__device__ __forceinline__ unsigned pk_bf16(float lo, float hi) {
  bf16x2 v = {(__bf16)lo, (__bf16)hi};
  return __builtin_bit_cast(unsigned, v);
}
__device__ __forceinline__ f32x4 mfma32(u32x4 a, u32x4 b, f32x4 c) {
  return __builtin_amdgcn_mfma_f32_16x16x32_bf16(__builtin_bit_cast(bf16x8, a), __builtin_bit_cast(bf16x8, b), c, 0, 0, 0);
}
__device__ __forceinline__ u32x4 make_srd(const void* base, unsigned nbytes) {
  const uint64_t a = (uint64_t)base;
  return u32x4{(unsigned)a, (unsigned)(a >> 32), nbytes, 0x00020000u};
}
__device__ __forceinline__ void load_k(u32x4 (&d)[8], int voff, u32x4 srd) {
  asm volatile("buffer_load_dwordx4 %0, %8, %9, 0 offen nt\n\t"
               "buffer_load_dwordx4 %1, %8, %9, 0 offen offset:128 nt\n\t"
               "buffer_load_dwordx4 %2, %8, %9, 0 offen offset:256 nt\n\t"
               "buffer_load_dwordx4 %3, %8, %9, 0 offen offset:384 nt\n\t"
               "buffer_load_dwordx4 %4, %8, %9, 0 offen offset:512 nt\n\t"
               "buffer_load_dwordx4 %5, %8, %9, 0 offen offset:640 nt\n\t"
               "buffer_load_dwordx4 %6, %8, %9, 0 offen offset:768 nt\n\t"
               "buffer_load_dwordx4 %7, %8, %9, 0 offen offset:896 nt"
               : "=&v"(d[0]), "=&v"(d[1]), "=&v"(d[2]), "=&v"(d[3]), "=&v"(d[4]), "=&v"(d[5]), "=&v"(d[6]), "=&v"(d[7])
               : "v"(voff), "s"(srd));
  __builtin_amdgcn_sched_barrier(0);
}
__device__ __forceinline__ void load_v(unsigned dst, const int (&vo)[4], u32x4 srd) {
  asm volatile("s_mov_b32 m0, %0\n\ts_nop 0\n\t"
               "buffer_load_dwordx4 %2, %6, 0 offen lds\n\t"
               "buffer_load_dwordx4 %3, %6, 0 offen offset:1024 lds\n\t"
               "buffer_load_dwordx4 %4, %6, 0 offen offset:2048 lds\n\t"
               "buffer_load_dwordx4 %5, %6, 0 offen offset:3072 lds\n\t"
               "s_mov_b32 m0, %1\n\ts_nop 0\n\t"
               "buffer_load_dwordx4 %2, %6, %7 offen lds\n\t"
               "buffer_load_dwordx4 %3, %6, %7 offen offset:1024 lds\n\t"
               "buffer_load_dwordx4 %4, %6, %7 offen offset:2048 lds\n\t"
               "buffer_load_dwordx4 %5, %6, %7 offen offset:3072 lds"
               :: "s"(dst), "s"(dst + 4096u), "v"(vo[0]), "v"(vo[1]), "v"(vo[2]), "v"(vo[3]), "s"(srd), "s"(4096)
               : "m0", "memory");
  __builtin_amdgcn_sched_barrier(0);
}
__device__ __forceinline__ unsigned hint_of(u32x4 t) { return 0x5a000000u | (t[0] >> 8) | (t[1] >> 4) | t[2] | (t[3] << 4); }
template <int N> __device__ __forceinline__ void wait_vm() { asm volatile("s_waitcnt vmcnt(%0)" :: "i"(N) : "memory"); }
__device__ __forceinline__ void wg_barrier() { asm volatile("s_waitcnt lgkmcnt(0)\n\ts_barrier" ::: "memory"); }
__device__ __forceinline__ int flag_get(LAS char* lds, int i) {
  return __builtin_amdgcn_readfirstlane(__hip_atomic_load((LAS int*)(lds + LDS_FL) + i, __ATOMIC_RELAXED, __HIP_MEMORY_SCOPE_WORKGROUP));
}
__device__ __forceinline__ void flag_add(LAS char* lds, int i, int) {
  asm volatile("s_waitcnt lgkmcnt(0)\n\t"
               "s_mov_b64 exec, 1\n\t"
               "ds_add_u32 %0, %1\n\t"
               "s_mov_b64 exec, -1"
               :: "v"((int)(uintptr_t)(lds + LDS_FL) + i * 4), "v"(1) : "memory");
}
__device__ __forceinline__ int flag_inc2(LAS char* lds, int a, int b) {
  int r;
  asm volatile("s_mov_b64 exec, 1\n\t"
               "ds_add_u32 %1, %3\n\t"
               "ds_add_rtn_u32 %0, %2, %3\n\t"
               "s_mov_b64 exec, -1\n\t"
               "s_waitcnt lgkmcnt(0)"
               : "=&v"(r) : "v"((int)(uintptr_t)(lds + LDS_FL) + a * 4), "v"((int)(uintptr_t)(lds + LDS_FL) + b * 4), "v"(1) : "memory");
  return __builtin_amdgcn_readfirstlane(r);
}
template <int SLEEP>
__device__ __forceinline__ void flag_wait(LAS char* lds, int i, int v) {
  for (unsigned spin = 0; flag_get(lds, i) < v && spin < (1u << 18); ++spin) __builtin_amdgcn_s_sleep(SLEEP);
  asm volatile("" ::: "memory");
}
#define LEAD 8
#define HINT_THR 232
#define PRETOUCH 0
#define LOOK 0
#define EARLY 8
#define ISSW 3

__global__ __launch_bounds__(NTHR, 4) void attn_decode(const uint16_t* __restrict__ q, const uint16_t* __restrict__ kc,
                                                       const uint16_t* __restrict__ vc, const int* __restrict__ bt,
                                                       const int* __restrict__ lens, char* __restrict__ ws,
                                                       uint16_t* __restrict__ out) {
  __shared__ __attribute__((aligned(1024))) char smem[LDS_BYTES];
  LAS char* lds = (LAS char*)smem;
  ST(0);
  const int tid = threadIdx.x, lane = tid & 63;
  const int wave = __builtin_amdgcn_readfirstlane(tid >> 6);
  const int g = lane >> 4, c = lane & 15;
  const bool lo = c < 8;
  const int wg = blockIdx.x;

  asm volatile("" ::"s"(ws), "s"(vc), "s"(kc), "s"(out), "s"(q));
  u32x4 tag;
#define LEN_DERIVED() \
  const bool uni = len_raw <= 0;                 \
  const int len = uni ? NB * BS : min(len_raw, NB * BS); \
  const int nW = (((len + 7) >> 3) - wg + NWG - 1) / NWG;           \
  const int nG = (nW + 3) >> 2

  if (wave < 4) {
    __builtin_amdgcn_s_setprio(3);
    int len_raw;
    {
      const int qoff = ((8 * wave + (c & 7)) * D + 32 * ((c >> 3) ^ (wave & 1)) + 8 * g) * 2;
      u32x4 q0, q1;
      int btv;
      asm volatile("global_load_dwordx4 %0, %4, %5\n\t"
                   "global_load_dwordx4 %1, %4, %5 offset:128\n\t"
                   "global_load_dword %2, %6, %7\n\t"
                   "global_load_dwordx4 %3, %8, %9"
                   : "=&v"(q0), "=&v"(q1), "=&v"(btv), "=&v"(tag)
                   : "v"(qoff), "s"(q), "v"(((wg >> 1) + 128 * (wave * 8 + (lane & 7))) * 4), "s"(bt), "v"((lane & 3) * 16), "s"(ws + WS_EPOCH));
      asm volatile("s_load_dword %0, %1, 0x0\n\ts_waitcnt lgkmcnt(0)" : "=&s"(len_raw) : "s"(lens));
      asm volatile("s_waitcnt vmcnt(0)" : "+v"(q0), "+v"(q1), "+v"(btv), "+v"(tag));
      *(LAS u32x4*)(lds + LDS_Q + wave * 2048 + lane * 16) = q0;
      *(LAS u32x4*)(lds + LDS_Q + wave * 2048 + 1024 + lane * 16) = q1;
      *(LAS int*)(lds + LDS_BT + (wave * 8 + (lane & 7)) * 4) = btv;
#pragma unroll
      for (int e = 0; e < 4; ++e) tag[e] = (tag[e] + 0x100u) & TAG_MASK;
      *(LAS u32x4*)(lds + LDS_TAG + (lane & 3) * 16) = tag;
      *((LAS int*)(lds + LDS_FL) + (lane & 31)) = (lane & 31) == F_NEXT ? NWORK : 0;
      ST(1);
      wg_barrier();
    }
    LEN_DERIVED();
    float M[2] = {-INFINITY, -INFINITY}, l[2] = {0.f, 0.f};
    f32x4 acc[2][2];
#pragma unroll
    for (int s = 0; s < 2; ++s)
#pragma unroll
      for (int hf = 0; hf < 2; ++hf) acc[s][hf] = f32x4{0.f, 0.f, 0.f, 0.f};
    int trb[2];
#pragma unroll
    for (int s = 0; s < 2; ++s)
      trb[s] = LDS_V + g * 8192 + (c >> 2) * 1024 + (2 * ((2 * wave + s) ^ ((c >> 2) | ((g & 1) << 2))) + ((c >> 1) & 1)) * 16 + (c & 1) * 8;
    unsigned low[2][4];
    float inv[2] = {0.f, 0.f};
#pragma unroll
    for (int hf = 0; hf < 2; ++hf)
#pragma unroll
      for (int e = 0; e < 4; ++e) low[hf][e] = tag[e] | ((0xff800000u >> (8 * e)) & 0xffu);
    u32x4 pn[2];
    float aln[2];
    auto stage_a = [&](int G) {
      flag_wait<1>(lds, F_CNT + 2 * G, min(4, nW - 4 * G));
      const bool live = 4 * G + g < nW;
      f32x4 s0[2], s1[2];
#pragma unroll
      for (int hf = 0; hf < 2; ++hf) {
        const int so = LDS_S + ((G & 3) * 4 + g) * 1024 + hf * 512 + c * 16;
        s0[hf] = *(const LAS f32x4*)(lds + so); s1[hf] = *(const LAS f32x4*)(lds + so + 256);
      }
#pragma unroll
      for (int hf = 0; hf < 2; ++hf) {
        float x[8];
#pragma unroll
        for (int e = 0; e < 4; ++e) { x[e] = live ? s0[hf][e] : -INFINITY; x[4 + e] = live ? s1[hf][e] : -INFINITY; }
        const float mx = fmaxf(fmaxf(fmaxf(x[0], x[1]), fmaxf(x[2], x[3])), fmaxf(fmaxf(x[4], x[5]), fmaxf(x[6], x[7])));
        const float mn = fmaxf(M[hf], xg_max(mx));
        aln[hf] = __builtin_amdgcn_exp2f(M[hf] - mn);
        M[hf] = mn;
        float ps = 0.f;
#pragma unroll
        for (int e = 0; e < 8; ++e) { x[e] = __builtin_amdgcn_exp2f(x[e] - mn); ps += x[e]; }
        l[hf] = l[hf] * aln[hf] + ps;
#pragma unroll
        for (int e = 0; e < 4; ++e) pn[hf][e] = pk_bf16(x[2 * e], x[2 * e + 1]);
      }
      if (G == nG - 1) {
#pragma unroll
        for (int hf = 0; hf < 2; ++hf) {
          const float L = xg_sum(l[hf]);
          inv[hf] = __builtin_amdgcn_rcpf(L);
          const unsigned lse = __float_as_uint(M[hf] + __builtin_amdgcn_logf(L));
#pragma unroll
          for (int e = 0; e < 4; ++e) low[hf][e] = tag[e] | ((lse >> (8 * e)) & 0xffu);
        }
      }
    };
    if (nG > 0) stage_a(0);
    for (int G = 0; G < nG; ++G) {
      u32x4 pe[2], po[2];
#pragma unroll
      for (int hf = 0; hf < 2; ++hf) {
#pragma unroll
        for (int e = 0; e < 4; ++e) { pe[hf][e] = lo ? pn[hf][e] : 0u; po[hf][e] = lo ? 0u : pn[hf][e]; }
#pragma unroll
        for (int s = 0; s < 2; ++s) acc[s][hf] *= aln[hf];
      }
      if (G + 1 < nG) stage_a(G + 1);
      flag_wait<0>(lds, F_CNT + 2 * G + 1, G < 4 ? 4 : min(4, nW - 4 * G));
      if (G == 0) ST(6);
      if (G == 2) ST(7);
      if (G == 4) ST(8);
      if (G == nG - 1) ST(9);
#pragma unroll
      for (int s = 0; s < 2; ++s)
#pragma unroll
        for (int hd = 0; hd < 4; ++hd) {
          const int o = trb[s] + (G & 3) * 32768 + hd * 256;
          const u32x2 a0 = __builtin_bit_cast(u32x2, __builtin_amdgcn_ds_read_tr16_b64_v4i16((LAS s16x4*)(lds + o)));
          const u32x2 a1 = __builtin_bit_cast(u32x2, __builtin_amdgcn_ds_read_tr16_b64_v4i16((LAS s16x4*)(lds + o + 4096)));
          acc[s][hd >> 1] = mfma32(u32x4{a0[0], a0[1], a1[0], a1[1]}, (hd & 1) ? po[hd >> 1] : pe[hd >> 1], acc[s][hd >> 1]);
        }
      flag_add(lds, F_DONE + G, lane);
    }
    ST(11);
    u32x4 msg[2][2];
#pragma unroll
    for (int hf = 0; hf < 2; ++hf)
#pragma unroll
      for (int s = 0; s < 2; ++s)
#pragma unroll
        for (int e = 0; e < 4; ++e) msg[s][hf][e] = ((__float_as_uint(acc[s][hf][e] * inv[hf]) + 0x800u) & DATA_MASK) | low[hf][e];
    const int moff = ((c >> 2) * 32 + 8 * wave + g) * INBOX_BYTES + wg * 64 + (c & 3) * 16;
    asm volatile("global_store_dwordx4 %0, %4, %8 sc1\n\t"
                 "global_store_dwordx4 %1, %5, %8 sc1\n\t"
                 "global_store_dwordx4 %2, %6, %8 sc1\n\t"
                 "global_store_dwordx4 %3, %7, %8 sc1\n\t"
                 "s_nop 1"
                 :: "v"(moff), "v"(moff + 128 * INBOX_BYTES), "v"(moff + 4 * INBOX_BYTES), "v"(moff + 132 * INBOX_BYTES),
                    "v"(msg[0][0]), "v"(msg[0][1]), "v"(msg[1][0]), "v"(msg[1][1]), "s"(ws) : "memory");
    flag_add(lds, F_PUB, lane);
    ST(13);
  } else {
    int len_raw, pg0;
    asm volatile("s_load_dword %0, %2, 0x0\n\t"
                 "s_load_dword %1, %3, 0x0\n\t"
                 "s_waitcnt lgkmcnt(0)"
                 : "=&s"(len_raw), "=&s"(pg0) : "s"(lens), "s"(bt + (wg >> 1) + 128 * (wave - 4)));
    ST(1);
    LEN_DERIVED();
    const int lenk = uni ? 0 : len;
    const int koff = (c >> 1) * 1024 + (c & 1) * 64 + g * 16;
    const char* kbase = (const char*)kc + (wg & 1) * 8192;
    const char* vbase = (const char*)vc + (wg & 1) * 8192;
    const unsigned vlds = (unsigned)(uintptr_t)(lds + LDS_V);
    const int first = min(nW, LEAD), nlate = nW - first;
    const int nV = nW < 16 ? 4 * nG : nW;
    const int ntask = nW + nV;
    u32x4 qf[8];
    constexpr float C2 = 0.088388347648318440f * 1.4426950408889634f;
    auto scores = [&](u32x4 (&kb)[8], int j) {
      asm volatile("s_waitcnt vmcnt(0)"
                   : "+v"(kb[0]), "+v"(kb[1]), "+v"(kb[2]), "+v"(kb[3]), "+v"(kb[4]), "+v"(kb[5]), "+v"(kb[6]), "+v"(kb[7])::"memory");
      const int tok0 = (wg + j * NWG) * 8;
      f32x4 d[4];
#pragma unroll
      for (int hd = 0; hd < 4; ++hd) {
        d[hd] = mfma32(kb[2 * hd], qf[2 * hd], f32x4{0.f, 0.f, 0.f, 0.f});
        d[hd] = mfma32(kb[2 * hd + 1], qf[2 * hd + 1], d[hd]);
      }
#pragma unroll
      for (int hf = 0; hf < 2; ++hf) {
        f32x2 sc;
#pragma unroll
        for (int a = 0; a < 2; ++a) {
          const float u = lo ? d[2 * hf][2 * a] : d[2 * hf + 1][2 * a];
          const float w = lo ? d[2 * hf + 1][2 * a + 1] : d[2 * hf][2 * a + 1];
          const float x = (u + dpp<ROR8>(w)) * C2;
          sc[a] = (tok0 + 2 * g + a < len) ? x : -INFINITY;
        }
        *(LAS f32x2*)(lds + LDS_S + (j & 15) * 1024 + hf * 512 + (g >> 1) * 256 + c * 16 + (g & 1) * 8) = sc;
      }
      flag_add(lds, F_CNT + 2 * (j >> 2), lane);
    };
    auto read_q = [&]() {
#pragma unroll
      for (int f = 0; f < 8; ++f) qf[f] = *(const LAS u32x4*)(lds + LDS_Q + f * 1024 + lane * 16);
    };
    int s = wave - 4;
    if (s < first && s < EARLY) {
      u32x4 kb[8];
      load_k(kb, koff, make_srd(kbase + (size_t)(unsigned)pg0 * PAGE_BYTES, max(lenk - (wg + s * NWG) * 8, 0) * 1024));
      wg_barrier();
      const int snext = flag_inc2(lds, F_FIN, F_NEXT);
      read_q();
      scores(kb, s);
      s = snext;
    } else {
      wg_barrier();
      read_q();
    }
    ST(2);
    while (s < ntask) {
      const int t = s - first;
      const bool isk = t < 0 || (t < 2 * nlate && (t & 1));
      const int j = t < 0 ? s : (t < 2 * nlate ? (t >> 1) + ((t & 1) ? first : 0) : t - nlate);
      if (j >= 16) flag_wait<1>(lds, F_DONE + (j >> 2) - 4, 4);
      const unsigned pg = __builtin_amdgcn_readfirstlane(*(const LAS int*)(lds + LDS_BT + j * 4));
      const int tok0 = (wg + j * NWG) * 8;
      flag_wait<0>(lds, F_FIN, s - ISSW + 1);
      if (isk) {
        u32x4 kb[8];
        load_k(kb, koff, make_srd(kbase + (size_t)pg * PAGE_BYTES, max(lenk - tok0, 0) * 1024));
        s = flag_inc2(lds, F_FIN, F_NEXT);
        scores(kb, j);
      } else {
        if (j < nW) {
          int vo[4];
#pragma unroll
          for (int t4 = 0; t4 < 4; ++t4) vo[t4] = (lane ^ ((t4 | ((j & 1) << 2)) << 1)) * 16;
          load_v(vlds + (j & 15) * 8192, vo, make_srd(vbase + (size_t)pg * PAGE_BYTES, (len - tok0) * 1024));
        } else {
#pragma unroll
          for (int k = 0; k < 8; ++k) *(LAS u32x4*)(lds + LDS_V + j * 8192 + k * 1024 + lane * 16) = u32x4{0u, 0u, 0u, 0u};
        }
        s = flag_inc2(lds, F_FIN, F_NEXT);
        wait_vm<0>();
        flag_add(lds, F_CNT + 2 * (j >> 2) + 1, lane);
      }
    }
    ST(5);
    if (wave >= 8) {
      tag = *(const LAS u32x4*)(lds + LDS_TAG + (lane & 3) * 16);
      flag_wait<1>(lds, F_FIN, ntask - LOOK);
      ST(12);
      const int ct = tid - 512;
      const char* inbox = ws + (size_t)wg * INBOX_BYTES;
      u32x4 pc[2];
      unsigned long long act[2] = {~0ull, ~0ull};
      bool watch = false;
      for (unsigned spin = 0; spin < SPIN_CAP; ++spin) {
        asm volatile("s_mov_b64 exec, %4\n\t"
                     "global_load_dwordx4 %0, %2, %6 sc1\n\t"
                     "s_mov_b64 exec, %5\n\t"
                     "global_load_dwordx4 %1, %3, %6 sc1\n\t"
                     "s_mov_b64 exec, -1\n\t"
                     "s_waitcnt vmcnt(0)"
                     : "+v"(pc[0]), "+v"(pc[1]) : "v"(ct * 16), "v"(ct * 16 + 8192), "s"(act[0]), "s"(act[1]), "s"(inbox) : "memory");
        unsigned miss = 0u;
#pragma unroll
        for (int k = 0; k < 2; ++k) {
          unsigned x = 0;
#pragma unroll
          for (int e = 0; e < 4; ++e) x |= pc[k][e] ^ tag[e];
          miss |= (x & TAG_MASK) ? (1u << k) : 0u;
        }
        miss |= __builtin_amdgcn_mov_dpp(miss, 0xB1, 0xf, 0xf, true);
        miss |= __builtin_amdgcn_mov_dpp(miss, 0x4E, 0xf, 0xf, true);
        const unsigned long long m0 = __builtin_amdgcn_ballot_w64(miss & 1u), m1 = __builtin_amdgcn_ballot_w64(miss & 2u);
        const int nmiss = __builtin_popcountll(m0) + __builtin_popcountll(m1);
        if (nmiss == 0) break;
        if (spin == 0) {
          flag_wait<1>(lds, F_PUB, 4);
          act[0] = m0; act[1] = m1;
        } else if (watch) {
          act[0] &= m0; act[1] &= m1;
          if ((act[0] | act[1]) == 0ull) { act[0] = m0; act[1] = m1; watch = false; }
        } else if (nmiss > POLL_T1) {
          act[0] = (m0 & -m0) * 15ull; act[1] = (m1 & -m1) * 15ull; watch = true;
        } else {
          act[0] = m0; act[1] = m1;
        }
      }
      ST(14);
      float ls[2];
#pragma unroll
      for (int k = 0; k < 2; ++k)
        ls[k] = __uint_as_float(__builtin_amdgcn_perm(__builtin_amdgcn_perm(pc[k][3], pc[k][2], 0x0c0c0400u), __builtin_amdgcn_perm(pc[k][1], pc[k][0], 0x0c0c0400u), 0x05040100u));
      float Mx = fmaxf(ls[0], ls[1]);
      Mx = fmaxf(Mx, dpp<ROR4>(Mx)); Mx = fmaxf(Mx, dpp<ROR8>(Mx)); Mx = xg_max(Mx);
      const float Ms = (Mx == -INFINITY) ? 0.f : Mx;
      const float w0 = __builtin_amdgcn_exp2f(ls[0] - Ms), w1 = __builtin_amdgcn_exp2f(ls[1] - Ms);
      float v[5];
#pragma unroll
      for (int e = 0; e < 4; ++e) v[e] = w0 * __uint_as_float(pc[0][e] & DATA_MASK) + w1 * __uint_as_float(pc[1][e] & DATA_MASK);
      v[4] = w0 + w1;
#pragma unroll
      for (int e = 0; e < 5; ++e) { v[e] += dpp<ROR4>(v[e]); v[e] += dpp<ROR8>(v[e]); v[e] = xg_sum(v[e]); }
      if (lane < 4) {
        *(LAS f32x4*)(lds + LDS_S + ((wave - 8) * 4 + lane) * 32) = f32x4{v[0], v[1], v[2], v[3]};
        *(LAS f32x2*)(lds + LDS_S + ((wave - 8) * 4 + lane) * 32 + 16) = f32x2{v[4], Mx};
      }
      wg_barrier();
    }
  }
  if (wave == 8) {
    const int r = lane & 31;
    const f32x4 o = *(const LAS f32x4*)(lds + LDS_S + r * 32);
    const f32x2 wm = *(const LAS f32x2*)(lds + LDS_S + r * 32 + 16);
    float MM = wm[1];
    MM = fmaxf(MM, dpp<ROR4>(MM)); MM = fmaxf(MM, dpp<ROR8>(MM)); MM = x16_max(MM);
    const float wt = __builtin_amdgcn_exp2f(wm[1] - MM);
    float u[5] = {o[0] * wt, o[1] * wt, o[2] * wt, o[3] * wt, wm[0] * wt};
#pragma unroll
    for (int e = 0; e < 5; ++e) { u[e] += dpp<ROR4>(u[e]); u[e] += dpp<ROR8>(u[e]); u[e] = x16_sum(u[e]); }
    if (lane < 4) {
      const float inv = 1.f / u[4];
      *reinterpret_cast<uint2*>(out + (4 * (wg >> 5) + lane) * D + (wg & 31) * 4) = make_uint2(pk_bf16(u[0] * inv, u[1] * inv), pk_bf16(u[2] * inv, u[3] * inv));
      if (wg == 0) *reinterpret_cast<u32x4*>(ws + WS_EPOCH + lane * 16) = tag;
    }
  }
  ST(10);
}
}

extern "C" void kernel_launch(void* const* d_in, const int* in_sizes, int n_in, void* d_out, int out_size, void* d_ws,
                              size_t ws_size, hipStream_t stream) {
  attn_decode<<<dim3(NWG), dim3(NTHR), 0, stream>>>((const uint16_t*)d_in[0], (const uint16_t*)d_in[1], (const uint16_t*)d_in[2],
                                                    (const int*)d_in[3], (const int*)d_in[4], (char*)d_ws, (uint16_t*)d_out);
}
```

```cpp
#include <hip/hip_runtime.h>
#include <hip/hip_bf16.h>
#include <stdint.h>

#define ST(i) do {} while (0)
#define POLL_T1 32
#define POLL_S1 16
#define POLL_S2 0
#define POLL_DEPTH 0
namespace {
constexpr int H = 32, HKV = 4, D = 128, BS = 16;
constexpr int NB = 4096;
constexpr int NWG = 256, NWAVE = 8, NTHR = NWAVE * 64, NGW = NWG * NWAVE;
constexpr int NU = NB * 2 / NGW;
constexpr int PAGE_BYTES = BS * HKV * D * 2;
static_assert(NU == 4, "written for 4 units per wave");
constexpr int REC = D;
constexpr int LDS_Q = 4 * H * REC;
constexpr int LDS_ML = LDS_Q + 2048;
constexpr int LDS_FLOATS = LDS_ML + H * 8;
constexpr int INBOX_BYTES = NWG * 64;
constexpr int WS_EPOCH = NWG * INBOX_BYTES;
constexpr unsigned TAG_MASK = 0x1fcu;
constexpr unsigned SPIN_CAP = 1u << 15;

using bf16x8 = __attribute__((ext_vector_type(8))) __bf16;
using bf16x2 = __attribute__((ext_vector_type(2))) __bf16;
using s16x4 = __attribute__((ext_vector_type(4))) short;
using f32x4 = __attribute__((ext_vector_type(4))) float;
using u32x4 = __attribute__((ext_vector_type(4))) unsigned;
using u32x2 = __attribute__((ext_vector_type(2))) unsigned;

template <int CTRL> __device__ __forceinline__ float dpp(float x) {
  return __builtin_bit_cast(float, __builtin_amdgcn_mov_dpp(__builtin_bit_cast(int, x), CTRL, 0xf, 0xf, true));
}
constexpr int ROR4 = 0x124, ROR8 = 0x128;
__device__ __forceinline__ float xg_max(float x) {
  auto s = __builtin_amdgcn_permlane16_swap(__float_as_uint(x), __float_as_uint(x), false, false);
  x = fmaxf(__uint_as_float(s[0]), __uint_as_float(s[1]));
  auto t = __builtin_amdgcn_permlane32_swap(__float_as_uint(x), __float_as_uint(x), false, false);
  return fmaxf(__uint_as_float(t[0]), __uint_as_float(t[1]));
}
__device__ __forceinline__ float x16_sum(float x) {
  auto s = __builtin_amdgcn_permlane16_swap(__float_as_uint(x), __float_as_uint(x), false, false);
  return __uint_as_float(s[0]) + __uint_as_float(s[1]);
}
__device__ __forceinline__ float x16_max(float x) {
  auto s = __builtin_amdgcn_permlane16_swap(__float_as_uint(x), __float_as_uint(x), false, false);
  return fmaxf(__uint_as_float(s[0]), __uint_as_float(s[1]));
}
__device__ __forceinline__ float xg_sum(float x) {
  x = x16_sum(x);
  auto t = __builtin_amdgcn_permlane32_swap(__float_as_uint(x), __float_as_uint(x), false, false);
  return __uint_as_float(t[0]) + __uint_as_float(t[1]);
}
__device__ __forceinline__ unsigned pk_bf16(float lo, float hi) {
  bf16x2 v = {(__bf16)lo, (__bf16)hi};
  return __builtin_bit_cast(unsigned, v);
}
__device__ __forceinline__ f32x4 mfma32(u32x4 a, u32x4 b, f32x4 c) {
  return __builtin_amdgcn_mfma_f32_16x16x32_bf16(__builtin_bit_cast(bf16x8, a), __builtin_bit_cast(bf16x8, b), c, 0, 0, 0);
}
__device__ __forceinline__ f32x4 mfma16(u32x2 a, u32x2 b, f32x4 c) {
  return __builtin_amdgcn_mfma_f32_16x16x16bf16_1k(__builtin_bit_cast(s16x4, a), __builtin_bit_cast(s16x4, b), c, 0, 0, 0);
}
__device__ __forceinline__ u32x4 make_srd(const void* base, unsigned nbytes) {
  const uint64_t a = (uint64_t)base;
  return u32x4{(unsigned)a, (unsigned)(a >> 32), nbytes, 0x00020000u};
}
template <int ODD>
__device__ __forceinline__ void load8(u32x4 (&d)[8], int voff, u32x4 srd) {
  asm volatile("buffer_load_dwordx4 %0, %8, %9, 0 offen nt\n\t"
               "buffer_load_dwordx4 %1, %8, %9, 0 offen offset:%11 nt\n\t"
               "buffer_load_dwordx4 %2, %8, %9, 0 offen offset:256 nt\n\t"
               "buffer_load_dwordx4 %3, %8, %9, 0 offen offset:%12 nt\n\t"
               "buffer_load_dwordx4 %4, %8, %9, %10 offen nt\n\t"
               "buffer_load_dwordx4 %5, %8, %9, %10 offen offset:%11 nt\n\t"
               "buffer_load_dwordx4 %6, %8, %9, %10 offen offset:256 nt\n\t"
               "buffer_load_dwordx4 %7, %8, %9, %10 offen offset:%12 nt"
               : "=&v"(d[0]), "=&v"(d[1]), "=&v"(d[2]), "=&v"(d[3]), "=&v"(d[4]), "=&v"(d[5]), "=&v"(d[6]), "=&v"(d[7])
               : "v"(voff), "s"(srd), "s"(8192), "i"(ODD), "i"(256 + ODD));
  __builtin_amdgcn_sched_barrier(0);
}
__device__ __forceinline__ void wait_groups(int x, u32x4 (&r)[8]) {
  asm volatile("s_cmp_gt_u32 %8, 2\n\ts_cbranch_scc1 3f\n\t"
               "s_cmp_gt_u32 %8, 1\n\ts_cbranch_scc1 2f\n\t"
               "s_cmp_gt_u32 %8, 0\n\ts_cbranch_scc1 1f\n\t"
               "s_waitcnt vmcnt(0)\n"
               "1:\n\ts_waitcnt vmcnt(8)\n"
               "2:\n\ts_waitcnt vmcnt(16)\n"
               "3:\n\ts_waitcnt vmcnt(24)"
               : "+v"(r[0]), "+v"(r[1]), "+v"(r[2]), "+v"(r[3]), "+v"(r[4]), "+v"(r[5]), "+v"(r[6]), "+v"(r[7])
               : "s"(x) : "scc");
}
template <int T>
__device__ __forceinline__ void wait_v(int n, u32x4 (&r)[8]) {
  asm volatile("s_cmp_gt_i32 %8, %9\n\ts_cbranch_scc1 1f\n\t"
               "s_waitcnt vmcnt(0)\n"
               "1:\n\ts_waitcnt vmcnt(8)"
               : "+v"(r[0]), "+v"(r[1]), "+v"(r[2]), "+v"(r[3]), "+v"(r[4]), "+v"(r[5]), "+v"(r[6]), "+v"(r[7])
               : "s"(n), "i"(T) : "scc");
}
__device__ __forceinline__ void wg_barrier() { asm volatile("s_waitcnt lgkmcnt(0)\n\ts_barrier" ::: "memory"); }

__global__ __launch_bounds__(NTHR, 2) void attn_decode(const uint16_t* __restrict__ q, const uint16_t* __restrict__ kc,
                                                       const uint16_t* __restrict__ vc, const int* __restrict__ bt,
                                                       const int* __restrict__ lens, char* __restrict__ ws,
                                                       uint16_t* __restrict__ out) {
  __shared__ __attribute__((aligned(16))) float lds[LDS_FLOATS];
  ST(0);
  const int tid = threadIdx.x, lane = tid & 63;
  const int wave = __builtin_amdgcn_readfirstlane(tid >> 6);
  const int g = lane >> 4, c = lane & 15;
  const bool lo = c < 8;
  const int wg = blockIdx.x;
  const int hf = wave >> 2, w4 = wave & 3;
  const int gp = w4 * NWG + ((wg + 128 * hf) & (NWG - 1));

  __builtin_amdgcn_s_setprio(3);
  asm volatile("" ::"s"(ws), "s"(vc), "s"(kc), "s"(out));
  int len_raw, pg[NU];
  u32x4 qtmp, tag;
  char* inbox = ws + (size_t)wg * INBOX_BYTES;
  {
    asm volatile("s_load_dword %0, %5, 0x0\n\t"
                 "s_load_dword %1, %6, 0x0\n\t"
                 "s_load_dword %2, %6, 0x1000\n\t"
                 "s_load_dword %3, %6, 0x2000\n\t"
                 "s_load_dword %4, %6, 0x3000"
                 : "=&s"(len_raw), "=&s"(pg[0]), "=&s"(pg[1]), "=&s"(pg[2]), "=&s"(pg[3])
                 : "s"(lens), "s"(bt + gp));
    const int hd = wave >> 1, ln = wave & 1;
    const int qoff = ((8 * hd + (c & 7)) * D + 64 * ln + 32 * ((c >> 3) ^ (hd & 1)) + 8 * g) * 2;
    asm volatile("global_load_dwordx4 %0, %2, %3\n\t"
                 "global_load_dwordx4 %1, %4, %5"
                 : "=&v"(qtmp), "=&v"(tag) : "v"(qoff), "s"(q), "v"((lane & 3) * 16), "s"(ws + WS_EPOCH));
    __builtin_amdgcn_sched_barrier(0);
    asm volatile("s_waitcnt lgkmcnt(0)" : "+s"(len_raw), "+s"(pg[0]), "+s"(pg[1]), "+s"(pg[2]), "+s"(pg[3]));
  }
  ST(1);
  const bool uni = len_raw <= 0;
  const int len = uni ? NB * BS : min(len_raw, NB * BS);
  const int lenk = uni ? 0 : len;
  const int n = (((len + 15) >> 4) - gp + 1023) >> 10;
  const int koff = (c >> 1) * 1024 + (c & 1) * 64 + g * 16;
  const int voff = g * 2048 + c * 16;
  const char* kbase = (const char*)kc + hf * 512;
  const char* vbase = (const char*)vc + hf * 512;

  u32x4 buf[NU][8];
  auto issue_k = [&](int i) {
    load8<128>(buf[i], koff, make_srd(kbase + (size_t)(unsigned)pg[i] * PAGE_BYTES, max(lenk - (gp + i * 1024) * 16, 0) * 1024));
  };
  auto issue_v = [&](int i) {
    load8<1024>(buf[i], voff, make_srd(vbase + (size_t)(unsigned)pg[i] * PAGE_BYTES, (len - (gp + i * 1024) * 16) * 1024));
  };
  if (0 < n) issue_k(0);
  asm volatile("s_cmp_gt_u32 %2, 0\n\ts_cbranch_scc1 1f\n\t"
               "s_waitcnt vmcnt(0)\n"
               "1:\n\ts_waitcnt vmcnt(8)"
               : "+v"(qtmp), "+v"(tag) : "s"(n) : "scc");
#pragma unroll
  for (int e = 0; e < 4; ++e) tag[e] = (tag[e] + 4u) & TAG_MASK;
  reinterpret_cast<u32x4*>(lds + LDS_Q)[tid] = qtmp;
  wg_barrier();
  ST(2);
  if (1 < n) issue_k(1);
  if (2 < n) issue_k(2);
  u32x4 qf[4];
#pragma unroll
  for (int f = 0; f < 4; ++f) qf[f] = reinterpret_cast<const u32x4*>(lds + LDS_Q)[(4 * hf + f) * 64 + lane];

  constexpr float C2 = 0.088388347648318440f * 1.4426950408889634f;
  float sc[NU][4];
#pragma unroll
  for (int i = 0; i < NU; ++i) {
    if (i < n) {
      const int tok0 = (gp + i * 1024) * 16;
      wait_groups(i == 0 ? min(n, 3) - 1 : i == 3 ? 2 : n - 1, buf[i]);
      ST(3 + i);
      f32x4 d[4];
#pragma unroll
      for (int e = 0; e < 4; ++e) {
        d[e] = mfma32(buf[i][2 * e], qf[2 * (e & 1)], f32x4{0.f, 0.f, 0.f, 0.f});
        d[e] = mfma32(buf[i][2 * e + 1], qf[2 * (e & 1) + 1], d[e]);
      }
      __builtin_amdgcn_sched_barrier(0);
      if (i == 0) { if (3 < n) issue_k(3); __builtin_amdgcn_s_setprio(2); issue_v(0); }
      if (i == 1) { __builtin_amdgcn_s_setprio(1); issue_v(1); }
#pragma unroll
      for (int o = 0; o < 2; ++o)
#pragma unroll
        for (int a = 0; a < 2; ++a) {
          const float u = lo ? d[2 * o][2 * a] : d[2 * o + 1][2 * a];
          const float w = lo ? d[2 * o + 1][2 * a + 1] : d[2 * o][2 * a + 1];
          const float s = (u + dpp<ROR8>(w)) * C2;
          sc[i][2 * o + a] = (tok0 + 8 * o + 2 * g + a < len) ? s : -INFINITY;
        }
    } else {
#pragma unroll
      for (int k = 0; k < 4; ++k) sc[i][k] = -INFINITY;
    }
  }
  u32x2 pf[NU];
  {
    float mx = -INFINITY;
#pragma unroll
    for (int i = 0; i < NU; ++i) mx = fmaxf(mx, fmaxf(fmaxf(sc[i][0], sc[i][1]), fmaxf(sc[i][2], sc[i][3])));
    mx = xg_max(mx);
    const float ms = (mx == -INFINITY) ? 0.f : mx;
    float ls = 0.f;
#pragma unroll
    for (int i = 0; i < NU; ++i) {
#pragma unroll
      for (int k = 0; k < 4; ++k) { sc[i][k] = __builtin_amdgcn_exp2f(sc[i][k] - ms); ls += sc[i][k]; }
      pf[i] = u32x2{pk_bf16(sc[i][0], sc[i][1]), pk_bf16(sc[i][2], sc[i][3])};
    }
    ls = xg_sum(ls);
    if (g == 0) { lds[LDS_ML + (16 * hf + c) * 8 + w4] = mx; lds[LDS_ML + (16 * hf + c) * 8 + 4 + w4] = ls; }
  }
  __builtin_amdgcn_s_setprio(0);
  f32x4 acc[8];
#pragma unroll
  for (int k = 0; k < 8; ++k) acc[k] = f32x4{0.f, 0.f, 0.f, 0.f};
#pragma unroll
  for (int i = 0; i < NU; ++i) {
    if (i < n) {
      if (i == 0) wait_v<1>(n, buf[0]);
      if (i == 1) wait_v<2>(n, buf[1]);
      if (i == 2) wait_v<3>(n, buf[2]);
      if (i == 3) wait_v<4>(n, buf[3]);
      if (i < 3) ST(7 + i);
      if (i + 2 < NU && i + 2 < n) issue_v(i + 2);
      const u32x4 b = {lo ? pf[i][0] : 0u, lo ? pf[i][1] : 0u, lo ? 0u : pf[i][0], lo ? 0u : pf[i][1]};
#pragma unroll
      for (int k = 0; k < 8; ++k) {
        const unsigned sel = (k & 1) ? 0x07060302u : 0x05040100u;
        const u32x4 a = {__builtin_amdgcn_perm(buf[i][1][k >> 1], buf[i][0][k >> 1], sel), __builtin_amdgcn_perm(buf[i][5][k >> 1], buf[i][4][k >> 1], sel),
                         __builtin_amdgcn_perm(buf[i][3][k >> 1], buf[i][2][k >> 1], sel), __builtin_amdgcn_perm(buf[i][7][k >> 1], buf[i][6][k >> 1], sel)};
        acc[k] = mfma32(a, b, acc[k]);
      }
    }
  }
  ST(11);
  u32x4 pc[2];
  asm volatile("global_load_dwordx4 %0, %2, %4 sc1\n\t"
               "global_load_dwordx4 %1, %3, %4 sc1"
               : "=&v"(pc[0]), "=&v"(pc[1]) : "v"(tid * 16), "v"(tid * 16 + 8192), "s"(inbox));
  {
    float* rec = lds + (w4 * H + 16 * hf + c) * REC;
#pragma unroll
    for (int k = 0; k < 8; ++k) *reinterpret_cast<f32x4*>(rec + 32 * g + 4 * (k ^ (c & 7))) = acc[k];
  }
  wg_barrier();
  ST(12);
  {
    const int h = tid >> 4, d4 = tid & 15;
    f32x4 po[2][4];
    const f32x4 mw = *reinterpret_cast<const f32x4*>(lds + LDS_ML + h * 8), lw = *reinterpret_cast<const f32x4*>(lds + LDS_ML + h * 8 + 4);
#pragma unroll
    for (int w = 0; w < 4; ++w)
#pragma unroll
      for (int k = 0; k < 2; ++k) po[k][w] = *reinterpret_cast<const f32x4*>(lds + (w * H + h) * REC + 4 * (d4 ^ (h & 7)) + 64 * k);
    __builtin_amdgcn_sched_barrier(0);
    const float M = fmaxf(fmaxf(mw[0], mw[1]), fmaxf(mw[2], mw[3]));
    const float Ms = (M == -INFINITY) ? 0.f : M;
    float L = 0.f, wt[4];
#pragma unroll
    for (int w = 0; w < 4; ++w) { wt[w] = __builtin_amdgcn_exp2f(mw[w] - Ms); L += wt[w] * lw[w]; }
    const float inv = L > 0.f ? __builtin_amdgcn_rcpf(L) : 0.f;
    const unsigned lse = __float_as_uint(M + __builtin_amdgcn_logf(L));
    unsigned low[4];
#pragma unroll
    for (int e = 0; e < 4; ++e) { low[e] = tag[e] | ((lse >> (8 * (d4 & 3) + 2 * e)) & 3u); wt[e] *= inv; }
    u32x4 msg[2];
#pragma unroll
    for (int k = 0; k < 2; ++k) {
      const f32x4 o = (wt[0] * po[k][0] + wt[1] * po[k][1]) + (wt[2] * po[k][2] + wt[3] * po[k][3]);
#pragma unroll
      for (int e = 0; e < 4; ++e) msg[k][e] = (__float_as_uint(o[e]) & 0xfffffe00u) | low[e];
    }
    const int moff = (h * 8 + (d4 >> 2)) * INBOX_BYTES + wg * 64 + (d4 & 3) * 16;
    asm volatile("global_store_dwordx4 %0, %2, %4 sc1\n\t"
                 "global_store_dwordx4 %1, %3, %4 sc1\n\t"
                 "s_nop 1"
                 :: "v"(moff), "v"(moff + 4 * INBOX_BYTES), "v"(msg[0]), "v"(msg[1]), "s"(ws) : "memory");
    asm volatile("s_waitcnt vmcnt(2)" : "+v"(pc[0]), "+v"(pc[1]));
  }
  ST(13);
  auto look = [&](unsigned long long a0, unsigned long long a1) {
    asm volatile("s_mov_b64 exec, %4\n\t"
                 "global_load_dwordx4 %0, %2, %6 sc1\n\t"
                 "s_mov_b64 exec, %5\n\t"
                 "global_load_dwordx4 %1, %3, %6 sc1\n\t"
                 "s_mov_b64 exec, -1"
                 : "+v"(pc[0]), "+v"(pc[1]) : "v"(tid * 16), "v"(tid * 16 + 8192), "s"(a0), "s"(a1), "s"(inbox) : "memory");
  };
  for (unsigned spin = 0; spin < SPIN_CAP; ++spin) {
    unsigned x0 = 0, x1 = 0;
#pragma unroll
    for (int e = 0; e < 4; ++e) { x0 |= pc[0][e] ^ tag[e]; x1 |= pc[1][e] ^ tag[e]; }
    x0 = (x0 & TAG_MASK) | ((x1 & TAG_MASK) << 16);
    x0 |= __builtin_amdgcn_mov_dpp(x0, 0xB1, 0xf, 0xf, true);
    x0 |= __builtin_amdgcn_mov_dpp(x0, 0x4E, 0xf, 0xf, true);
    const unsigned long long m0 = __builtin_amdgcn_ballot_w64((x0 & 0xffffu) != 0u), m1 = __builtin_amdgcn_ballot_w64((x0 >> 16) != 0u);
    const int nmiss = __builtin_popcountll(m0) + __builtin_popcountll(m1);
    if (nmiss == 0) break;
    if (nmiss > POLL_T1) __builtin_amdgcn_s_sleep(POLL_S1);
    else __builtin_amdgcn_s_sleep(POLL_S2);
    look(m0, m1);
    asm volatile("s_waitcnt vmcnt(%2)" : "+v"(pc[0]), "+v"(pc[1]) : "i"(POLL_DEPTH));
  }
  u32x4 keep[2] = {pc[0], pc[1]};
  asm volatile("" : "=v"(pc[0]), "=v"(pc[1]) : "0"(pc[0]), "1"(pc[1]));
  ST(14);
  {
    float* mrg = lds + LDS_Q;
    float ls[2];
#pragma unroll
    for (int k = 0; k < 2; ++k) {
      unsigned b = ((pc[k][0] & 3u) | ((pc[k][1] & 3u) << 2) | ((pc[k][2] & 3u) << 4) | ((pc[k][3] & 3u) << 6)) << (8 * (lane & 3));
      b |= __builtin_amdgcn_mov_dpp(b, 0xB1, 0xf, 0xf, true);
      b |= __builtin_amdgcn_mov_dpp(b, 0x4E, 0xf, 0xf, true);
      ls[k] = __uint_as_float(b);
    }
    float M = fmaxf(ls[0], ls[1]);
    M = fmaxf(M, dpp<ROR4>(M)); M = fmaxf(M, dpp<ROR8>(M)); M = xg_max(M);
    const float Ms = (M == -INFINITY) ? 0.f : M;
    const float w0 = __builtin_amdgcn_exp2f(ls[0] - Ms), w1 = __builtin_amdgcn_exp2f(ls[1] - Ms);
    float v[5];
#pragma unroll
    for (int e = 0; e < 4; ++e) v[e] = w0 * __uint_as_float(pc[0][e] & 0xfffffe00u) + w1 * __uint_as_float(pc[1][e] & 0xfffffe00u);
    v[4] = w0 + w1;
#pragma unroll
    for (int e = 0; e < 5; ++e) { v[e] += dpp<ROR4>(v[e]); v[e] += dpp<ROR8>(v[e]); v[e] = xg_sum(v[e]); }
    if (lane < 4) {
      *reinterpret_cast<f32x4*>(mrg + (wave * 4 + lane) * 8) = f32x4{v[0], v[1], v[2], v[3]};
      *reinterpret_cast<float2*>(mrg + (wave * 4 + lane) * 8 + 4) = make_float2(v[4], M);
    }
    wg_barrier();
    if (wave == 0) {
      const int r = lane & 31;
      f32x4 o = *reinterpret_cast<const f32x4*>(mrg + r * 8);
      const float2 wm = *reinterpret_cast<const float2*>(mrg + r * 8 + 4);
      float MM = wm.y;
      MM = fmaxf(MM, dpp<ROR4>(MM)); MM = fmaxf(MM, dpp<ROR8>(MM)); MM = x16_max(MM);
      const float wt = __builtin_amdgcn_exp2f(wm.y - MM);
      float u[5] = {o[0] * wt, o[1] * wt, o[2] * wt, o[3] * wt, wm.x * wt};
#pragma unroll
      for (int e = 0; e < 5; ++e) { u[e] += dpp<ROR4>(u[e]); u[e] += dpp<ROR8>(u[e]); u[e] = x16_sum(u[e]); }
      if (lane < 4) {
        const float inv = 1.f / u[4];
        uint16_t* o16 = out + (wg >> 3) * D + 32 * ((wg & 7) >> 1) + 4 * (wg & 1) + lane;
        const unsigned p01 = pk_bf16(u[0] * inv, u[1] * inv), p23 = pk_bf16(u[2] * inv, u[3] * inv);
        o16[0] = (uint16_t)p01; o16[8] = (uint16_t)(p01 >> 16); o16[16] = (uint16_t)p23; o16[24] = (uint16_t)(p23 >> 16);
      }
      if (wg == 0 && lane < 4) *reinterpret_cast<u32x4*>(ws + WS_EPOCH + lane * 16) = tag;
    }
  }
  asm volatile("s_waitcnt vmcnt(0)" :: "v"(keep[0]), "v"(keep[1]));
  ST(10);
}

}

extern "C" void kernel_launch(void* const* d_in, const int* in_sizes, int n_in, void* d_out, int out_size, void* d_ws,
                              size_t ws_size, hipStream_t stream) {
  attn_decode<<<dim3(NWG), dim3(NTHR), 0, stream>>>((const uint16_t*)d_in[0], (const uint16_t*)d_in[1], (const uint16_t*)d_in[2],
                                                   (const int*)d_in[3], (const int*)d_in[4], (char*)d_ws, (uint16_t*)d_out);
}
```

```cpp
#include <hip/hip_runtime.h>
#include <hip/hip_bf16.h>
#include <stdint.h>

#define ST(i) do {} while (0)
#define POLL_T1 32
#define POLL_S1 16
#define POLL_S2 0
#define POLL_DEPTH 0
#define HSHIFT 128
namespace {
constexpr int H = 32, HKV = 4, D = 128, BS = 16;
constexpr int NB = 4096;
constexpr int NWG = 256, NWAVE = 8, NTHR = NWAVE * 64, NGW = NWG * NWAVE;
constexpr int NU = NB * 2 / NGW;
constexpr int PAGE_BYTES = BS * HKV * D * 2;
static_assert(NU == 4, "written for 4 units per wave");
constexpr int REC = D;
constexpr int LDS_Q = 4 * H * REC;
constexpr int LDS_ML = LDS_Q + 2048;
constexpr int LDS_FLOATS = LDS_ML + H * 8;
constexpr int INBOX_BYTES = NWG * 64;
constexpr int WS_EPOCH = NWG * INBOX_BYTES;
constexpr unsigned TAG_MASK = 0x1fcu;
constexpr unsigned SPIN_CAP = 1u << 15;

using bf16x8 = __attribute__((ext_vector_type(8))) __bf16;
using bf16x2 = __attribute__((ext_vector_type(2))) __bf16;
using s16x4 = __attribute__((ext_vector_type(4))) short;
using f32x4 = __attribute__((ext_vector_type(4))) float;
using u32x4 = __attribute__((ext_vector_type(4))) unsigned;
using u32x2 = __attribute__((ext_vector_type(2))) unsigned;

template <int CTRL> __device__ __forceinline__ float dpp(float x) {
  return __builtin_bit_cast(float, __builtin_amdgcn_mov_dpp(__builtin_bit_cast(int, x), CTRL, 0xf, 0xf, true));
}
constexpr int ROR4 = 0x124, ROR8 = 0x128;
__device__ __forceinline__ float xg_max(float x) {
  auto s = __builtin_amdgcn_permlane16_swap(__float_as_uint(x), __float_as_uint(x), false, false);
  x = fmaxf(__uint_as_float(s[0]), __uint_as_float(s[1]));
  auto t = __builtin_amdgcn_permlane32_swap(__float_as_uint(x), __float_as_uint(x), false, false);
  return fmaxf(__uint_as_float(t[0]), __uint_as_float(t[1]));
}
__device__ __forceinline__ float x16_sum(float x) {
  auto s = __builtin_amdgcn_permlane16_swap(__float_as_uint(x), __float_as_uint(x), false, false);
  return __uint_as_float(s[0]) + __uint_as_float(s[1]);
}
__device__ __forceinline__ float x16_max(float x) {
  auto s = __builtin_amdgcn_permlane16_swap(__float_as_uint(x), __float_as_uint(x), false, false);
  return fmaxf(__uint_as_float(s[0]), __uint_as_float(s[1]));
}
__device__ __forceinline__ float xg_sum(float x) {
  x = x16_sum(x);
  auto t = __builtin_amdgcn_permlane32_swap(__float_as_uint(x), __float_as_uint(x), false, false);
  return __uint_as_float(t[0]) + __uint_as_float(t[1]);
}
__device__ __forceinline__ unsigned pk_bf16(float lo, float hi) {
  bf16x2 v = {(__bf16)lo, (__bf16)hi};
  return __builtin_bit_cast(unsigned, v);
}
__device__ __forceinline__ f32x4 mfma32(u32x4 a, u32x4 b, f32x4 c) {
  return __builtin_amdgcn_mfma_f32_16x16x32_bf16(__builtin_bit_cast(bf16x8, a), __builtin_bit_cast(bf16x8, b), c, 0, 0, 0);
}
__device__ __forceinline__ f32x4 mfma16(u32x2 a, u32x2 b, f32x4 c) {
  return __builtin_amdgcn_mfma_f32_16x16x16bf16_1k(__builtin_bit_cast(s16x4, a), __builtin_bit_cast(s16x4, b), c, 0, 0, 0);
}
__device__ __forceinline__ u32x4 make_srd(const void* base, unsigned nbytes) {
  const uint64_t a = (uint64_t)base;
  return u32x4{(unsigned)a, (unsigned)(a >> 32), nbytes, 0x00020000u};
}
template <int ODD>
__device__ __forceinline__ void load8(u32x4 (&d)[8], int voff, u32x4 srd) {
  asm volatile("buffer_load_dwordx4 %0, %8, %9, 0 offen nt\n\t"
               "buffer_load_dwordx4 %1, %8, %9, 0 offen offset:%11 nt\n\t"
               "buffer_load_dwordx4 %2, %8, %9, 0 offen offset:256 nt\n\t"
               "buffer_load_dwordx4 %3, %8, %9, 0 offen offset:%12 nt\n\t"
               "buffer_load_dwordx4 %4, %8, %9, %10 offen nt\n\t"
               "buffer_load_dwordx4 %5, %8, %9, %10 offen offset:%11 nt\n\t"
               "buffer_load_dwordx4 %6, %8, %9, %10 offen offset:256 nt\n\t"
               "buffer_load_dwordx4 %7, %8, %9, %10 offen offset:%12 nt"
               : "=&v"(d[0]), "=&v"(d[1]), "=&v"(d[2]), "=&v"(d[3]), "=&v"(d[4]), "=&v"(d[5]), "=&v"(d[6]), "=&v"(d[7])
               : "v"(voff), "s"(srd), "s"(8192), "i"(ODD), "i"(256 + ODD));
  __builtin_amdgcn_sched_barrier(0);
}
__device__ __forceinline__ void wait_groups(int x, u32x4 (&r)[8]) {
  asm volatile("s_cmp_gt_u32 %8, 2\n\ts_cbranch_scc1 3f\n\t"
               "s_cmp_gt_u32 %8, 1\n\ts_cbranch_scc1 2f\n\t"
               "s_cmp_gt_u32 %8, 0\n\ts_cbranch_scc1 1f\n\t"
               "s_waitcnt vmcnt(0)\n"
               "1:\n\ts_waitcnt vmcnt(8)\n"
               "2:\n\ts_waitcnt vmcnt(16)\n"
               "3:\n\ts_waitcnt vmcnt(24)"
               : "+v"(r[0]), "+v"(r[1]), "+v"(r[2]), "+v"(r[3]), "+v"(r[4]), "+v"(r[5]), "+v"(r[6]), "+v"(r[7])
               : "s"(x) : "scc");
}
template <int T>
__device__ __forceinline__ void wait_v(int n, u32x4 (&r)[8]) {
  asm volatile("s_cmp_gt_i32 %8, %9\n\ts_cbranch_scc1 1f\n\t"
               "s_waitcnt vmcnt(0)\n"
               "1:\n\ts_waitcnt vmcnt(8)"
               : "+v"(r[0]), "+v"(r[1]), "+v"(r[2]), "+v"(r[3]), "+v"(r[4]), "+v"(r[5]), "+v"(r[6]), "+v"(r[7])
               : "s"(n), "i"(T) : "scc");
}
__device__ __forceinline__ void wg_barrier() { asm volatile("s_waitcnt lgkmcnt(0)\n\ts_barrier" ::: "memory"); }

__global__ __launch_bounds__(NTHR, 2) void attn_decode(const uint16_t* __restrict__ q, const uint16_t* __restrict__ kc,
                                                       const uint16_t* __restrict__ vc, const int* __restrict__ bt,
                                                       const int* __restrict__ lens, char* __restrict__ ws,
                                                       uint16_t* __restrict__ out) {
  __shared__ __attribute__((aligned(16))) float lds[LDS_FLOATS];
  ST(0);
  const int tid = threadIdx.x, lane = tid & 63;
  const int wave = __builtin_amdgcn_readfirstlane(tid >> 6);
  const int g = lane >> 4, c = lane & 15;
  const bool lo = c < 8;
  const int wg = blockIdx.x;
  const int hf = wave >> 2, w4 = wave & 3;
  const int gp = w4 * NWG + ((wg + HSHIFT * hf) & (NWG - 1));

  __builtin_amdgcn_s_setprio(3);
  asm volatile("" ::"s"(ws), "s"(vc), "s"(kc), "s"(out));
  int len_raw, pg[NU];
  u32x4 qtmp, tag;
  char* inbox = ws + (size_t)wg * INBOX_BYTES;
  {
    asm volatile("s_load_dword %0, %5, 0x0\n\t"
                 "s_load_dword %1, %6, 0x0\n\t"
                 "s_load_dword %2, %6, 0x1000\n\t"
                 "s_load_dword %3, %6, 0x2000\n\t"
                 "s_load_dword %4, %6, 0x3000"
                 : "=&s"(len_raw), "=&s"(pg[0]), "=&s"(pg[1]), "=&s"(pg[2]), "=&s"(pg[3])
                 : "s"(lens), "s"(bt + gp));
    const int hd = wave >> 1, ln = wave & 1;
    const int qoff = ((8 * hd + (c & 7)) * D + 64 * ln + 32 * ((c >> 3) ^ (hd & 1)) + 8 * g) * 2;
    asm volatile("global_load_dwordx4 %0, %2, %3\n\t"
                 "global_load_dwordx4 %1, %4, %5"
                 : "=&v"(qtmp), "=&v"(tag) : "v"(qoff), "s"(q), "v"((lane & 3) * 16), "s"(ws + WS_EPOCH));
    __builtin_amdgcn_sched_barrier(0);
    asm volatile("s_waitcnt lgkmcnt(0)" : "+s"(len_raw), "+s"(pg[0]), "+s"(pg[1]), "+s"(pg[2]), "+s"(pg[3]));
  }
  ST(1);
  const bool uni = len_raw <= 0;
  const int len = uni ? NB * BS : min(len_raw, NB * BS);
  const int lenk = uni ? 0 : len;
  const int n = (((len + 15) >> 4) - gp + 1023) >> 10;
  const int koff = (c >> 1) * 1024 + (c & 1) * 64 + g * 16;
  const int voff = g * 2048 + c * 16;
  const char* kbase = (const char*)kc + hf * 512;
  const char* vbase = (const char*)vc + hf * 512;

  u32x4 buf[NU][8];
  auto issue_k = [&](int i) {
    load8<128>(buf[i], koff, make_srd(kbase + (size_t)(unsigned)pg[i] * PAGE_BYTES, max(lenk - (gp + i * 1024) * 16, 0) * 1024));
  };
  auto issue_v = [&](int i) {
    load8<1024>(buf[i], voff, make_srd(vbase + (size_t)(unsigned)pg[i] * PAGE_BYTES, (len - (gp + i * 1024) * 16) * 1024));
  };
  if (0 < n) issue_k(0);
  asm volatile("s_cmp_gt_u32 %2, 0\n\ts_cbranch_scc1 1f\n\t"
               "s_waitcnt vmcnt(0)\n"
               "1:\n\ts_waitcnt vmcnt(8)"
               : "+v"(qtmp), "+v"(tag) : "s"(n) : "scc");
#pragma unroll
  for (int e = 0; e < 4; ++e) tag[e] = (tag[e] + 4u) & TAG_MASK;
  reinterpret_cast<u32x4*>(lds + LDS_Q)[tid] = qtmp;
  wg_barrier();
  ST(2);
  if (1 < n) issue_k(1);
  if (2 < n) issue_k(2);
  u32x4 qf[4];
#pragma unroll
  for (int f = 0; f < 4; ++f) qf[f] = reinterpret_cast<const u32x4*>(lds + LDS_Q)[(4 * hf + f) * 64 + lane];

  constexpr float C2 = 0.088388347648318440f * 1.4426950408889634f;
  float sc[NU][4];
#pragma unroll
  for (int i = 0; i < NU; ++i) {
    if (i < n) {
      const int tok0 = (gp + i * 1024) * 16;
      wait_groups(i == 0 ? min(n, 3) - 1 : i == 3 ? 2 : n - 1, buf[i]);
      ST(3 + i);
      f32x4 d[4];
#pragma unroll
      for (int e = 0; e < 4; ++e) {
        d[e] = mfma32(buf[i][2 * e], qf[2 * (e & 1)], f32x4{0.f, 0.f, 0.f, 0.f});
        d[e] = mfma32(buf[i][2 * e + 1], qf[2 * (e & 1) + 1], d[e]);
      }
      __builtin_amdgcn_sched_barrier(0);
      if (i == 0) { if (3 < n) issue_k(3); else __builtin_amdgcn_s_setprio(2); issue_v(0); }
      if (i == 1) { if (3 < n) __builtin_amdgcn_s_setprio(2); else __builtin_amdgcn_s_setprio(1); issue_v(1); }
#pragma unroll
      for (int o = 0; o < 2; ++o)
#pragma unroll
        for (int a = 0; a < 2; ++a) {
          const float u = lo ? d[2 * o][2 * a] : d[2 * o + 1][2 * a];
          const float w = lo ? d[2 * o + 1][2 * a + 1] : d[2 * o][2 * a + 1];
          const float s = (u + dpp<ROR8>(w)) * C2;
          sc[i][2 * o + a] = (tok0 + 8 * o + 2 * g + a < len) ? s : -INFINITY;
        }
    } else {
#pragma unroll
      for (int k = 0; k < 4; ++k) sc[i][k] = -INFINITY;
    }
  }
  u32x2 pf[NU];
  {
    float mx = -INFINITY;
#pragma unroll
    for (int i = 0; i < NU; ++i) mx = fmaxf(mx, fmaxf(fmaxf(sc[i][0], sc[i][1]), fmaxf(sc[i][2], sc[i][3])));
    mx = xg_max(mx);
    const float ms = (mx == -INFINITY) ? 0.f : mx;
    float ls = 0.f;
#pragma unroll
    for (int i = 0; i < NU; ++i) {
#pragma unroll
      for (int k = 0; k < 4; ++k) { sc[i][k] = __builtin_amdgcn_exp2f(sc[i][k] - ms); ls += sc[i][k]; }
      pf[i] = u32x2{pk_bf16(sc[i][0], sc[i][1]), pk_bf16(sc[i][2], sc[i][3])};
    }
    ls = xg_sum(ls);
    if (g == 0) { lds[LDS_ML + (16 * hf + c) * 8 + w4] = mx; lds[LDS_ML + (16 * hf + c) * 8 + 4 + w4] = ls; }
  }
  if (3 < n) __builtin_amdgcn_s_setprio(1); else __builtin_amdgcn_s_setprio(0);
  f32x4 acc[8];
#pragma unroll
  for (int k = 0; k < 8; ++k) acc[k] = f32x4{0.f, 0.f, 0.f, 0.f};
#pragma unroll
  for (int i = 0; i < NU; ++i) {
    if (i < n) {
      if (i == 0) wait_v<1>(n, buf[0]);
      if (i == 1) wait_v<2>(n, buf[1]);
      if (i == 2) wait_v<3>(n, buf[2]);
      if (i == 3) wait_v<4>(n, buf[3]);
      if (i < 3) ST(7 + i);
      if (i + 2 < NU && i + 2 < n) issue_v(i + 2);
      const u32x4 b = {lo ? pf[i][0] : 0u, lo ? pf[i][1] : 0u, lo ? 0u : pf[i][0], lo ? 0u : pf[i][1]};
#pragma unroll
      for (int k = 0; k < 8; ++k) {
        const unsigned sel = (k & 1) ? 0x07060302u : 0x05040100u;
        const u32x4 a = {__builtin_amdgcn_perm(buf[i][1][k >> 1], buf[i][0][k >> 1], sel), __builtin_amdgcn_perm(buf[i][5][k >> 1], buf[i][4][k >> 1], sel),
                         __builtin_amdgcn_perm(buf[i][3][k >> 1], buf[i][2][k >> 1], sel), __builtin_amdgcn_perm(buf[i][7][k >> 1], buf[i][6][k >> 1], sel)};
        acc[k] = mfma32(a, b, acc[k]);
      }
    }
  }
  ST(11);
  u32x4 pc[2];
  asm volatile("global_load_dwordx4 %0, %2, %4 sc1\n\t"
               "global_load_dwordx4 %1, %3, %4 sc1"
               : "=&v"(pc[0]), "=&v"(pc[1]) : "v"(tid * 16), "v"(tid * 16 + 8192), "s"(inbox));
  {
    float* rec = lds + (w4 * H + 16 * hf + c) * REC;
#pragma unroll
    for (int k = 0; k < 8; ++k) *reinterpret_cast<f32x4*>(rec + 32 * g + 4 * (k ^ (c & 7))) = acc[k];
  }
  wg_barrier();
  ST(12);
  {
    const int h = tid >> 4, d4 = tid & 15;
    f32x4 po[2][4];
    const f32x4 mw = *reinterpret_cast<const f32x4*>(lds + LDS_ML + h * 8), lw = *reinterpret_cast<const f32x4*>(lds + LDS_ML + h * 8 + 4);
#pragma unroll
    for (int w = 0; w < 4; ++w)
#pragma unroll
      for (int k = 0; k < 2; ++k) po[k][w] = *reinterpret_cast<const f32x4*>(lds + (w * H + h) * REC + 4 * (d4 ^ (h & 7)) + 64 * k);
    __builtin_amdgcn_sched_barrier(0);
    const float M = fmaxf(fmaxf(mw[0], mw[1]), fmaxf(mw[2], mw[3]));
    const float Ms = (M == -INFINITY) ? 0.f : M;
    float L = 0.f, wt[4];
#pragma unroll
    for (int w = 0; w < 4; ++w) { wt[w] = __builtin_amdgcn_exp2f(mw[w] - Ms); L += wt[w] * lw[w]; }
    const float inv = L > 0.f ? __builtin_amdgcn_rcpf(L) : 0.f;
    const unsigned lse = __float_as_uint(M + __builtin_amdgcn_logf(L));
    unsigned low[4];
#pragma unroll
    for (int e = 0; e < 4; ++e) { low[e] = tag[e] | ((lse >> (8 * (d4 & 3) + 2 * e)) & 3u); wt[e] *= inv; }
    u32x4 msg[2];
#pragma unroll
    for (int k = 0; k < 2; ++k) {
      const f32x4 o = (wt[0] * po[k][0] + wt[1] * po[k][1]) + (wt[2] * po[k][2] + wt[3] * po[k][3]);
#pragma unroll
      for (int e = 0; e < 4; ++e) msg[k][e] = (__float_as_uint(o[e]) & 0xfffffe00u) | low[e];
    }
    const int moff = (h * 8 + (d4 >> 2)) * INBOX_BYTES + wg * 64 + (d4 & 3) * 16;
    asm volatile("global_store_dwordx4 %0, %2, %4 sc1\n\t"
                 "global_store_dwordx4 %1, %3, %4 sc1\n\t"
                 "s_nop 1"
                 :: "v"(moff), "v"(moff + 4 * INBOX_BYTES), "v"(msg[0]), "v"(msg[1]), "s"(ws) : "memory");
    asm volatile("s_waitcnt vmcnt(2)" : "+v"(pc[0]), "+v"(pc[1]));
  }
  ST(13);
  auto look = [&](unsigned long long a0, unsigned long long a1) {
    asm volatile("s_mov_b64 exec, %4\n\t"
                 "global_load_dwordx4 %0, %2, %6 sc1\n\t"
                 "s_mov_b64 exec, %5\n\t"
                 "global_load_dwordx4 %1, %3, %6 sc1\n\t"
                 "s_mov_b64 exec, -1"
                 : "+v"(pc[0]), "+v"(pc[1]) : "v"(tid * 16), "v"(tid * 16 + 8192), "s"(a0), "s"(a1), "s"(inbox) : "memory");
  };
  for (unsigned spin = 0; spin < SPIN_CAP; ++spin) {
    unsigned x0 = 0, x1 = 0;
#pragma unroll
    for (int e = 0; e < 4; ++e) { x0 |= pc[0][e] ^ tag[e]; x1 |= pc[1][e] ^ tag[e]; }
    x0 = (x0 & TAG_MASK) | ((x1 & TAG_MASK) << 16);
    x0 |= __builtin_amdgcn_mov_dpp(x0, 0xB1, 0xf, 0xf, true);
    x0 |= __builtin_amdgcn_mov_dpp(x0, 0x4E, 0xf, 0xf, true);
    const unsigned long long m0 = __builtin_amdgcn_ballot_w64((x0 & 0xffffu) != 0u), m1 = __builtin_amdgcn_ballot_w64((x0 >> 16) != 0u);
    const int nmiss = __builtin_popcountll(m0) + __builtin_popcountll(m1);
    if (nmiss == 0) break;
    if (nmiss > POLL_T1) __builtin_amdgcn_s_sleep(POLL_S1);
    else __builtin_amdgcn_s_sleep(POLL_S2);
    look(m0, m1);
    asm volatile("s_waitcnt vmcnt(%2)" : "+v"(pc[0]), "+v"(pc[1]) : "i"(POLL_DEPTH));
  }
  u32x4 keep[2] = {pc[0], pc[1]};
  asm volatile("" : "=v"(pc[0]), "=v"(pc[1]) : "0"(pc[0]), "1"(pc[1]));
  ST(14);
  {
    float* mrg = lds + LDS_Q;
    float ls[2];
#pragma unroll
    for (int k = 0; k < 2; ++k) {
      unsigned b = ((pc[k][0] & 3u) | ((pc[k][1] & 3u) << 2) | ((pc[k][2] & 3u) << 4) | ((pc[k][3] & 3u) << 6)) << (8 * (lane & 3));
      b |= __builtin_amdgcn_mov_dpp(b, 0xB1, 0xf, 0xf, true);
      b |= __builtin_amdgcn_mov_dpp(b, 0x4E, 0xf, 0xf, true);
      ls[k] = __uint_as_float(b);
    }
    float M = fmaxf(ls[0], ls[1]);
    M = fmaxf(M, dpp<ROR4>(M)); M = fmaxf(M, dpp<ROR8>(M)); M = xg_max(M);
    const float Ms = (M == -INFINITY) ? 0.f : M;
    const float w0 = __builtin_amdgcn_exp2f(ls[0] - Ms), w1 = __builtin_amdgcn_exp2f(ls[1] - Ms);
    float v[5];
#pragma unroll
    for (int e = 0; e < 4; ++e) v[e] = w0 * __uint_as_float(pc[0][e] & 0xfffffe00u) + w1 * __uint_as_float(pc[1][e] & 0xfffffe00u);
    v[4] = w0 + w1;
#pragma unroll
    for (int e = 0; e < 5; ++e) { v[e] += dpp<ROR4>(v[e]); v[e] += dpp<ROR8>(v[e]); v[e] = xg_sum(v[e]); }
    if (lane < 4) {
      *reinterpret_cast<f32x4*>(mrg + (wave * 4 + lane) * 8) = f32x4{v[0], v[1], v[2], v[3]};
      *reinterpret_cast<float2*>(mrg + (wave * 4 + lane) * 8 + 4) = make_float2(v[4], M);
    }
    wg_barrier();
    if (wave == 0) {
      const int r = lane & 31;
      f32x4 o = *reinterpret_cast<const f32x4*>(mrg + r * 8);
      const float2 wm = *reinterpret_cast<const float2*>(mrg + r * 8 + 4);
      float MM = wm.y;
      MM = fmaxf(MM, dpp<ROR4>(MM)); MM = fmaxf(MM, dpp<ROR8>(MM)); MM = x16_max(MM);
      const float wt = __builtin_amdgcn_exp2f(wm.y - MM);
      float u[5] = {o[0] * wt, o[1] * wt, o[2] * wt, o[3] * wt, wm.x * wt};
#pragma unroll
      for (int e = 0; e < 5; ++e) { u[e] += dpp<ROR4>(u[e]); u[e] += dpp<ROR8>(u[e]); u[e] = x16_sum(u[e]); }
      if (lane < 4) {
        const float inv = 1.f / u[4];
        uint16_t* o16 = out + (wg >> 3) * D + 32 * ((wg & 7) >> 1) + 4 * (wg & 1) + lane;
        const unsigned p01 = pk_bf16(u[0] * inv, u[1] * inv), p23 = pk_bf16(u[2] * inv, u[3] * inv);
        o16[0] = (uint16_t)p01; o16[8] = (uint16_t)(p01 >> 16); o16[16] = (uint16_t)p23; o16[24] = (uint16_t)(p23 >> 16);
      }
      if (wg == 0 && lane < 4) *reinterpret_cast<u32x4*>(ws + WS_EPOCH + lane * 16) = tag;
    }
  }
  asm volatile("s_waitcnt vmcnt(0)" :: "v"(keep[0]), "v"(keep[1]));
  ST(10);
}

}

extern "C" void kernel_launch(void* const* d_in, const int* in_sizes, int n_in, void* d_out, int out_size, void* d_ws,
                              size_t ws_size, hipStream_t stream) {
  attn_decode<<<dim3(NWG), dim3(NTHR), 0, stream>>>((const uint16_t*)d_in[0], (const uint16_t*)d_in[1], (const uint16_t*)d_in[2],
                                                   (const int*)d_in[3], (const int*)d_in[4], (char*)d_ws, (uint16_t*)d_out);
}
```

```cpp
#include <hip/hip_runtime.h>
#include <hip/hip_bf16.h>
#include <stdint.h>

#define ST(i) do {} while (0)
#define POLL_T1 16
#define POLL_S1 16
#define POLL_S2 0
#define POLL_DEPTH 0
#define HSHIFT 128
namespace {
constexpr int H = 32, HKV = 4, D = 128, BS = 16;
constexpr int NB = 4096;
constexpr int NWG = 256, NWAVE = 8, NTHR = NWAVE * 64, NGW = NWG * NWAVE;
constexpr int NU = NB * 2 / NGW;
constexpr int PAGE_BYTES = BS * HKV * D * 2;
static_assert(NU == 4, "written for 4 units per wave");
constexpr int REC = D;
constexpr int LDS_Q = 4 * H * REC;
constexpr int LDS_ML = LDS_Q + 2048;
constexpr int LDS_FLOATS = LDS_ML + 16 * 16;
constexpr int INBOX_BYTES = 128 * 64;
constexpr int WS_EPOCH = NWG * INBOX_BYTES;
constexpr unsigned TAG_MASK = 0x1fcu;
constexpr unsigned SPIN_CAP = 1u << 15;

using bf16x8 = __attribute__((ext_vector_type(8))) __bf16;
using bf16x2 = __attribute__((ext_vector_type(2))) __bf16;
using s16x4 = __attribute__((ext_vector_type(4))) short;
using f32x4 = __attribute__((ext_vector_type(4))) float;
using u32x4 = __attribute__((ext_vector_type(4))) unsigned;
using u32x2 = __attribute__((ext_vector_type(2))) unsigned;

template <int CTRL> __device__ __forceinline__ float dpp(float x) {
  return __builtin_bit_cast(float, __builtin_amdgcn_mov_dpp(__builtin_bit_cast(int, x), CTRL, 0xf, 0xf, true));
}
constexpr int ROR4 = 0x124, ROR8 = 0x128;
__device__ __forceinline__ float xg_max(float x) {
  auto s = __builtin_amdgcn_permlane16_swap(__float_as_uint(x), __float_as_uint(x), false, false);
  x = fmaxf(__uint_as_float(s[0]), __uint_as_float(s[1]));
  auto t = __builtin_amdgcn_permlane32_swap(__float_as_uint(x), __float_as_uint(x), false, false);
  return fmaxf(__uint_as_float(t[0]), __uint_as_float(t[1]));
}
__device__ __forceinline__ float x16_sum(float x) {
  auto s = __builtin_amdgcn_permlane16_swap(__float_as_uint(x), __float_as_uint(x), false, false);
  return __uint_as_float(s[0]) + __uint_as_float(s[1]);
}
__device__ __forceinline__ float x16_max(float x) {
  auto s = __builtin_amdgcn_permlane16_swap(__float_as_uint(x), __float_as_uint(x), false, false);
  return fmaxf(__uint_as_float(s[0]), __uint_as_float(s[1]));
}
__device__ __forceinline__ float xg_sum(float x) {
  x = x16_sum(x);
  auto t = __builtin_amdgcn_permlane32_swap(__float_as_uint(x), __float_as_uint(x), false, false);
  return __uint_as_float(t[0]) + __uint_as_float(t[1]);
}
__device__ __forceinline__ unsigned pk_bf16(float lo, float hi) {
  bf16x2 v = {(__bf16)lo, (__bf16)hi};
  return __builtin_bit_cast(unsigned, v);
}
__device__ __forceinline__ f32x4 mfma32(u32x4 a, u32x4 b, f32x4 c) {
  return __builtin_amdgcn_mfma_f32_16x16x32_bf16(__builtin_bit_cast(bf16x8, a), __builtin_bit_cast(bf16x8, b), c, 0, 0, 0);
}
__device__ __forceinline__ f32x4 mfma16(u32x2 a, u32x2 b, f32x4 c) {
  return __builtin_amdgcn_mfma_f32_16x16x16bf16_1k(__builtin_bit_cast(s16x4, a), __builtin_bit_cast(s16x4, b), c, 0, 0, 0);
}
__device__ __forceinline__ u32x4 make_srd(const void* base, unsigned nbytes) {
  const uint64_t a = (uint64_t)base;
  return u32x4{(unsigned)a, (unsigned)(a >> 32), nbytes, 0x00020000u};
}
template <int ODD>
__device__ __forceinline__ void load8(u32x4 (&d)[8], int voff, u32x4 srd) {
  asm volatile("buffer_load_dwordx4 %0, %8, %9, 0 offen nt\n\t"
               "buffer_load_dwordx4 %1, %8, %9, 0 offen offset:%11 nt\n\t"
               "buffer_load_dwordx4 %2, %8, %9, 0 offen offset:256 nt\n\t"
               "buffer_load_dwordx4 %3, %8, %9, 0 offen offset:%12 nt\n\t"
               "buffer_load_dwordx4 %4, %8, %9, %10 offen nt\n\t"
               "buffer_load_dwordx4 %5, %8, %9, %10 offen offset:%11 nt\n\t"
               "buffer_load_dwordx4 %6, %8, %9, %10 offen offset:256 nt\n\t"
               "buffer_load_dwordx4 %7, %8, %9, %10 offen offset:%12 nt"
               : "=&v"(d[0]), "=&v"(d[1]), "=&v"(d[2]), "=&v"(d[3]), "=&v"(d[4]), "=&v"(d[5]), "=&v"(d[6]), "=&v"(d[7])
               : "v"(voff), "s"(srd), "s"(8192), "i"(ODD), "i"(256 + ODD));
  __builtin_amdgcn_sched_barrier(0);
}
__device__ __forceinline__ void wait_groups(int x, u32x4 (&r)[8]) {
  asm volatile("s_cmp_gt_u32 %8, 2\n\ts_cbranch_scc1 3f\n\t"
               "s_cmp_gt_u32 %8, 1\n\ts_cbranch_scc1 2f\n\t"
               "s_cmp_gt_u32 %8, 0\n\ts_cbranch_scc1 1f\n\t"
               "s_waitcnt vmcnt(0)\n"
               "1:\n\ts_waitcnt vmcnt(8)\n"
               "2:\n\ts_waitcnt vmcnt(16)\n"
               "3:\n\ts_waitcnt vmcnt(24)"
               : "+v"(r[0]), "+v"(r[1]), "+v"(r[2]), "+v"(r[3]), "+v"(r[4]), "+v"(r[5]), "+v"(r[6]), "+v"(r[7])
               : "s"(x) : "scc");
}
template <int T>
__device__ __forceinline__ void wait_v(int n, u32x4 (&r)[8]) {
  asm volatile("s_cmp_gt_i32 %8, %9\n\ts_cbranch_scc1 1f\n\t"
               "s_waitcnt vmcnt(0)\n"
               "1:\n\ts_waitcnt vmcnt(8)"
               : "+v"(r[0]), "+v"(r[1]), "+v"(r[2]), "+v"(r[3]), "+v"(r[4]), "+v"(r[5]), "+v"(r[6]), "+v"(r[7])
               : "s"(n), "i"(T) : "scc");
}
__device__ __forceinline__ void wg_barrier() { asm volatile("s_waitcnt lgkmcnt(0)\n\ts_barrier" ::: "memory"); }

__global__ __launch_bounds__(NTHR, 2) void attn_decode(const uint16_t* __restrict__ q, const uint16_t* __restrict__ kc,
                                                       const uint16_t* __restrict__ vc, const int* __restrict__ bt,
                                                       const int* __restrict__ lens, char* __restrict__ ws,
                                                       uint16_t* __restrict__ out) {
  __shared__ __attribute__((aligned(16))) float lds[LDS_FLOATS];
  ST(0);
  const int tid = threadIdx.x, lane = tid & 63;
  const int wave = __builtin_amdgcn_readfirstlane(tid >> 6);
  const int g = lane >> 4, c = lane & 15;
  const bool lo = c < 8;
  const int wg = blockIdx.x;
    const int hf = wg >> 7, wi = wg & 127;
  const int gp = wave * 128 + wi;

  __builtin_amdgcn_s_setprio(3);
  asm volatile("" ::"s"(ws), "s"(vc), "s"(kc), "s"(out));
  int len_raw, pg[NU];
  u32x4 qtmp, tag;
  char* inbox = ws + (size_t)wg * INBOX_BYTES;
  {
    asm volatile("s_load_dword %0, %5, 0x0\n\t"
                 "s_load_dword %1, %6, 0x0\n\t"
                 "s_load_dword %2, %6, 0x1000\n\t"
                 "s_load_dword %3, %6, 0x2000\n\t"
                 "s_load_dword %4, %6, 0x3000"
                 : "=&s"(len_raw), "=&s"(pg[0]), "=&s"(pg[1]), "=&s"(pg[2]), "=&s"(pg[3])
                 : "s"(lens), "s"(bt + gp));
    const int hd = wave >> 1, ln = wave & 1;
    const int qoff = ((8 * hd + (c & 7)) * D + 64 * ln + 32 * ((c >> 3) ^ (hd & 1)) + 8 * g) * 2;
    asm volatile("global_load_dwordx4 %0, %2, %3\n\t"
                 "global_load_dwordx4 %1, %4, %5"
                 : "=&v"(qtmp), "=&v"(tag) : "v"(qoff), "s"(q), "v"((lane & 3) * 16), "s"(ws + WS_EPOCH));
    __builtin_amdgcn_sched_barrier(0);
    asm volatile("s_waitcnt lgkmcnt(0)" : "+s"(len_raw), "+s"(pg[0]), "+s"(pg[1]), "+s"(pg[2]), "+s"(pg[3]));
  }
  ST(1);
  const bool uni = len_raw <= 0;
  const int len = uni ? NB * BS : min(len_raw, NB * BS);
  const int lenk = uni ? 0 : len;
  const int n = (((len + 15) >> 4) - gp + 1023) >> 10;
  const int koff = (c >> 1) * 1024 + (c & 1) * 64 + g * 16;
  const int voff = g * 2048 + c * 16;
  const char* kbase = (const char*)kc + hf * 512;
  const char* vbase = (const char*)vc + hf * 512;

  u32x4 buf[NU][8];
  auto issue_k = [&](int i) {
    load8<128>(buf[i], koff, make_srd(kbase + (size_t)(unsigned)pg[i] * PAGE_BYTES, max(lenk - (gp + i * 1024) * 16, 0) * 1024));
  };
  auto issue_v = [&](int i) {
    load8<1024>(buf[i], voff, make_srd(vbase + (size_t)(unsigned)pg[i] * PAGE_BYTES, (len - (gp + i * 1024) * 16) * 1024));
  };
  if (0 < n) issue_k(0);
  asm volatile("s_cmp_gt_u32 %2, 0\n\ts_cbranch_scc1 1f\n\t"
               "s_waitcnt vmcnt(0)\n"
               "1:\n\ts_waitcnt vmcnt(8)"
               : "+v"(qtmp), "+v"(tag) : "s"(n) : "scc");
#pragma unroll
  for (int e = 0; e < 4; ++e) {
    const unsigned t = (tag[e] & TAG_MASK) + 4u;
    tag[e] = t >= 4u * (e == 0 ? 128 : e == 1 ? 127 : e == 2 ? 125 : 123) ? 0u : t;
  }
  reinterpret_cast<u32x4*>(lds + LDS_Q)[tid] = qtmp;
  wg_barrier();
  ST(2);
  if (1 < n) issue_k(1);
  if (2 < n) issue_k(2);
  u32x4 qf[4];
#pragma unroll
  for (int f = 0; f < 4; ++f) qf[f] = reinterpret_cast<const u32x4*>(lds + LDS_Q)[(4 * hf + f) * 64 + lane];

  constexpr float C2 = 0.088388347648318440f * 1.4426950408889634f;
  float sc[NU][4];
#pragma unroll
  for (int i = 0; i < NU; ++i) {
    if (i < n) {
      const int tok0 = (gp + i * 1024) * 16;
      wait_groups(i == 0 ? min(n, 3) - 1 : i == 3 ? 2 : n - 1, buf[i]);
      ST(3 + i);
      f32x4 d[4];
#pragma unroll
      for (int e = 0; e < 4; ++e) {
        d[e] = mfma32(buf[i][2 * e], qf[2 * (e & 1)], f32x4{0.f, 0.f, 0.f, 0.f});
        d[e] = mfma32(buf[i][2 * e + 1], qf[2 * (e & 1) + 1], d[e]);
      }
      __builtin_amdgcn_sched_barrier(0);
      if (i == 0) { if (3 < n) issue_k(3); else __builtin_amdgcn_s_setprio(2); issue_v(0); }
      if (i == 1) { if (3 < n) __builtin_amdgcn_s_setprio(2); else __builtin_amdgcn_s_setprio(1); issue_v(1); }
#pragma unroll
      for (int o = 0; o < 2; ++o)
#pragma unroll
        for (int a = 0; a < 2; ++a) {
          const float u = lo ? d[2 * o][2 * a] : d[2 * o + 1][2 * a];
          const float w = lo ? d[2 * o + 1][2 * a + 1] : d[2 * o][2 * a + 1];
          const float s = (u + dpp<ROR8>(w)) * C2;
          sc[i][2 * o + a] = (tok0 + 8 * o + 2 * g + a < len) ? s : -INFINITY;
        }
    } else {
#pragma unroll
      for (int k = 0; k < 4; ++k) sc[i][k] = -INFINITY;
    }
  }
  u32x2 pf[NU];
  {
    float mx = -INFINITY;
#pragma unroll
    for (int i = 0; i < NU; ++i) mx = fmaxf(mx, fmaxf(fmaxf(sc[i][0], sc[i][1]), fmaxf(sc[i][2], sc[i][3])));
    mx = xg_max(mx);
    const float ms = (mx == -INFINITY) ? 0.f : mx;
    float ls = 0.f;
#pragma unroll
    for (int i = 0; i < NU; ++i) {
#pragma unroll
      for (int k = 0; k < 4; ++k) { sc[i][k] = __builtin_amdgcn_exp2f(sc[i][k] - ms); ls += sc[i][k]; }
      pf[i] = u32x2{pk_bf16(sc[i][0], sc[i][1]), pk_bf16(sc[i][2], sc[i][3])};
    }
    ls = xg_sum(ls);
    if (g == 0) { lds[LDS_ML + c * 16 + wave] = mx; lds[LDS_ML + c * 16 + 8 + wave] = ls; }
  }
  if (3 < n) __builtin_amdgcn_s_setprio(1); else __builtin_amdgcn_s_setprio(0);
  f32x4 acc[8];
#pragma unroll
  for (int k = 0; k < 8; ++k) acc[k] = f32x4{0.f, 0.f, 0.f, 0.f};
#pragma unroll
  for (int i = 0; i < NU; ++i) {
    if (i < n) {
      if (i == 0) wait_v<1>(n, buf[0]);
      if (i == 1) wait_v<2>(n, buf[1]);
      if (i == 2) wait_v<3>(n, buf[2]);
      if (i == 3) wait_v<4>(n, buf[3]);
      if (i < 3) ST(7 + i);
      if (i + 2 < NU && i + 2 < n) issue_v(i + 2);
      const u32x4 b = {lo ? pf[i][0] : 0u, lo ? pf[i][1] : 0u, lo ? 0u : pf[i][0], lo ? 0u : pf[i][1]};
#pragma unroll
      for (int k = 0; k < 8; ++k) {
        const unsigned sel = (k & 1) ? 0x07060302u : 0x05040100u;
        const u32x4 a = {__builtin_amdgcn_perm(buf[i][1][k >> 1], buf[i][0][k >> 1], sel), __builtin_amdgcn_perm(buf[i][5][k >> 1], buf[i][4][k >> 1], sel),
                         __builtin_amdgcn_perm(buf[i][3][k >> 1], buf[i][2][k >> 1], sel), __builtin_amdgcn_perm(buf[i][7][k >> 1], buf[i][6][k >> 1], sel)};
        acc[k] = mfma32(a, b, acc[k]);
      }
    }
  }
  ST(11);
  u32x4 pc;
  asm volatile("global_load_dwordx4 %0, %1, %2 sc1" : "=&v"(pc) : "v"(tid * 16), "s"(inbox));
  {
    float* rec = lds + (wave * 16 + c) * REC;
#pragma unroll
    for (int k = 0; k < 8; ++k) *reinterpret_cast<f32x4*>(rec + 32 * g + 4 * (k ^ (c & 7))) = acc[k];
  }
  wg_barrier();
  ST(12);
  {
    const int h = tid >> 5, d4 = tid & 31;
    f32x4 po[NWAVE], mw[2], lw[2];
    const float* ml = lds + LDS_ML + h * 16;
    mw[0] = *reinterpret_cast<const f32x4*>(ml); mw[1] = *reinterpret_cast<const f32x4*>(ml + 4);
    lw[0] = *reinterpret_cast<const f32x4*>(ml + 8); lw[1] = *reinterpret_cast<const f32x4*>(ml + 12);
#pragma unroll
    for (int w = 0; w < NWAVE; ++w) po[w] = *reinterpret_cast<const f32x4*>(lds + (w * 16 + h) * REC + 4 * (d4 ^ (h & 7)));
    __builtin_amdgcn_sched_barrier(0);
    float M = -INFINITY;
#pragma unroll
    for (int w = 0; w < NWAVE; ++w) M = fmaxf(M, mw[w >> 2][w & 3]);
    const float Ms = (M == -INFINITY) ? 0.f : M;
    float L = 0.f, wt[NWAVE];
#pragma unroll
    for (int w = 0; w < NWAVE; ++w) { wt[w] = __builtin_amdgcn_exp2f(mw[w >> 2][w & 3] - Ms); L += wt[w] * lw[w >> 2][w & 3]; }
    const float inv = L > 0.f ? __builtin_amdgcn_rcpf(L) : 0.f;
    const unsigned lse = __float_as_uint(M + __builtin_amdgcn_logf(L));
    f32x4 o = ((wt[0] * po[0] + wt[1] * po[1]) + (wt[2] * po[2] + wt[3] * po[3])) + ((wt[4] * po[4] + wt[5] * po[5]) + (wt[6] * po[6] + wt[7] * po[7]));
    u32x4 msg;
#pragma unroll
    for (int e = 0; e < 4; ++e) msg[e] = (__float_as_uint(o[e] * inv) & 0xfffffe00u) | tag[e] | ((lse >> (8 * (d4 & 3) + 2 * e)) & 3u);
    const int moff = ((16 * hf + h) * 8 + (d4 >> 2)) * INBOX_BYTES + wi * 64 + (d4 & 3) * 16;
    asm volatile("global_store_dwordx4 %0, %1, %2 sc1\n\ts_nop 1" :: "v"(moff), "v"(msg), "s"(ws) : "memory");
    asm volatile("s_waitcnt vmcnt(1)" : "+v"(pc));
  }
  ST(13);
  for (unsigned spin = 0; spin < SPIN_CAP; ++spin) {
    unsigned x0 = 0;
#pragma unroll
    for (int e = 0; e < 4; ++e) x0 |= pc[e] ^ tag[e];
    x0 &= TAG_MASK;
    x0 |= __builtin_amdgcn_mov_dpp(x0, 0xB1, 0xf, 0xf, true);
    x0 |= __builtin_amdgcn_mov_dpp(x0, 0x4E, 0xf, 0xf, true);
    const unsigned long long m0 = __builtin_amdgcn_ballot_w64(x0 != 0u);
    if (m0 == 0ull) break;
    if (__builtin_popcountll(m0) > POLL_T1) __builtin_amdgcn_s_sleep(POLL_S1);
    asm volatile("s_mov_b64 exec, %2\n\t"
                 "global_load_dwordx4 %0, %1, %3 sc1\n\t"
                 "s_mov_b64 exec, -1\n\t"
                 "s_waitcnt vmcnt(0)"
                 : "+v"(pc) : "v"(tid * 16), "s"(m0), "s"(inbox) : "memory");
  }
  if (wg == 128 && wave >= 6) {
    for (unsigned spin = 0; spin < SPIN_CAP; ++spin) {
      u32x4 w;
      asm volatile("global_load_dwordx4 %0, %1, %2 sc1\n\ts_waitcnt vmcnt(0)" : "=&v"(w) : "v"((tid - 384) * 64 + (lane & 3) * 16), "s"(ws) : "memory");
      unsigned x = 0;
#pragma unroll
      for (int e = 0; e < 4; ++e) x |= w[e] ^ tag[e];
      if (__builtin_amdgcn_ballot_w64((x & TAG_MASK) != 0u) == 0ull) break;
    }
  }
  ST(14);
  {
    float* mrg = lds + LDS_Q;
    unsigned b = ((pc[0] & 3u) | ((pc[1] & 3u) << 2) | ((pc[2] & 3u) << 4) | ((pc[3] & 3u) << 6)) << (8 * (lane & 3));
    b |= __builtin_amdgcn_mov_dpp(b, 0xB1, 0xf, 0xf, true);
    b |= __builtin_amdgcn_mov_dpp(b, 0x4E, 0xf, 0xf, true);
    const float ls = __uint_as_float(b);
    float M = ls;
    M = fmaxf(M, dpp<ROR4>(M)); M = fmaxf(M, dpp<ROR8>(M)); M = xg_max(M);
    const float Ms = (M == -INFINITY) ? 0.f : M;
    const float w0 = __builtin_amdgcn_exp2f(ls - Ms);
    float v[5];
#pragma unroll
    for (int e = 0; e < 4; ++e) v[e] = w0 * __uint_as_float(pc[e] & 0xfffffe00u);
    v[4] = w0;
#pragma unroll
    for (int e = 0; e < 5; ++e) { v[e] += dpp<ROR4>(v[e]); v[e] += dpp<ROR8>(v[e]); v[e] = xg_sum(v[e]); }
    if (lane < 4) {
      *reinterpret_cast<f32x4*>(mrg + (wave * 4 + lane) * 8) = f32x4{v[0], v[1], v[2], v[3]};
      *reinterpret_cast<float2*>(mrg + (wave * 4 + lane) * 8 + 4) = make_float2(v[4], M);
    }
    wg_barrier();
    if (wave == 0) {
      const int r = lane & 31;
      f32x4 o = *reinterpret_cast<const f32x4*>(mrg + r * 8);
      const float2 wm = *reinterpret_cast<const float2*>(mrg + r * 8 + 4);
      float MM = wm.y;
      MM = fmaxf(MM, dpp<ROR4>(MM)); MM = fmaxf(MM, dpp<ROR8>(MM)); MM = x16_max(MM);
      const float wt = __builtin_amdgcn_exp2f(wm.y - MM);
      float u[5] = {o[0] * wt, o[1] * wt, o[2] * wt, o[3] * wt, wm.x * wt};
#pragma unroll
      for (int e = 0; e < 5; ++e) { u[e] += dpp<ROR4>(u[e]); u[e] += dpp<ROR8>(u[e]); u[e] = x16_sum(u[e]); }
      if (lane < 4) {
        const float inv = 1.f / u[4];
        uint16_t* o16 = out + (wg >> 3) * D + 32 * ((wg & 7) >> 1) + 4 * (wg & 1) + lane;
        const unsigned p01 = pk_bf16(u[0] * inv, u[1] * inv), p23 = pk_bf16(u[2] * inv, u[3] * inv);
        o16[0] = (uint16_t)p01; o16[8] = (uint16_t)(p01 >> 16); o16[16] = (uint16_t)p23; o16[24] = (uint16_t)(p23 >> 16);
      }
      if (wg == 128 && lane < 4) *reinterpret_cast<u32x4*>(ws + WS_EPOCH + lane * 16) = tag;
    }
  }
  ST(10);
}

}

extern "C" void kernel_launch(void* const* d_in, const int* in_sizes, int n_in, void* d_out, int out_size, void* d_ws,
                              size_t ws_size, hipStream_t stream) {
  attn_decode<<<dim3(NWG), dim3(NTHR), 0, stream>>>((const uint16_t*)d_in[0], (const uint16_t*)d_in[1], (const uint16_t*)d_in[2],
                                                   (const int*)d_in[3], (const int*)d_in[4], (char*)d_ws, (uint16_t*)d_out);
}
```

```cpp
#include <hip/hip_runtime.h>
#include <hip/hip_bf16.h>
#include <stdint.h>

#define ST(i) do {} while (0)

namespace {
constexpr int HKV = 4, D = 128, BS = 16;
constexpr int NB = 4096;
constexpr int NWG = 256, NWAVE = 8, NTHR = NWAVE * 64;
constexpr int NU = NB / (NWG / 2 * NWAVE);
constexpr int PAGE_BYTES = BS * HKV * D * 2;
static_assert(NU == 4, "written for 4 pages per wave");
constexpr int LDS_Q = NWAVE * 16 * D;
constexpr int LDS_ML = LDS_Q + 2048;
constexpr int LDS_FLOATS = LDS_ML + 16 * 16;
constexpr int INBOX_BYTES = 128 * 64;
constexpr int WS_EPOCH = NWG * INBOX_BYTES;
constexpr unsigned TAG_MASK = 0x1fcu, DATA_MASK = 0xfffffe00u;
constexpr unsigned SPIN_CAP = 1u << 15;

using bf16x8 = __attribute__((ext_vector_type(8))) __bf16;
using bf16x2 = __attribute__((ext_vector_type(2))) __bf16;
using f32x4 = __attribute__((ext_vector_type(4))) float;
using f32x2 = __attribute__((ext_vector_type(2))) float;
using u32x4 = __attribute__((ext_vector_type(4))) unsigned;
using u32x2 = __attribute__((ext_vector_type(2))) unsigned;

template <int CTRL> __device__ __forceinline__ float dpp(float x) {
  return __builtin_bit_cast(float, __builtin_amdgcn_mov_dpp(__builtin_bit_cast(int, x), CTRL, 0xf, 0xf, true));
}
constexpr int ROR4 = 0x124, ROR8 = 0x128, QX1 = 0xB1, QX2 = 0x4E;
__device__ __forceinline__ float x16_sum(float x) {
  auto s = __builtin_amdgcn_permlane16_swap(__float_as_uint(x), __float_as_uint(x), false, false);
  return __uint_as_float(s[0]) + __uint_as_float(s[1]);
}
__device__ __forceinline__ float x16_max(float x) {
  auto s = __builtin_amdgcn_permlane16_swap(__float_as_uint(x), __float_as_uint(x), false, false);
  return fmaxf(__uint_as_float(s[0]), __uint_as_float(s[1]));
}
__device__ __forceinline__ float xg_max(float x) {
  x = x16_max(x);
  auto t = __builtin_amdgcn_permlane32_swap(__float_as_uint(x), __float_as_uint(x), false, false);
  return fmaxf(__uint_as_float(t[0]), __uint_as_float(t[1]));
}
__device__ __forceinline__ float xg_sum(float x) {
  x = x16_sum(x);
  auto t = __builtin_amdgcn_permlane32_swap(__float_as_uint(x), __float_as_uint(x), false, false);
  return __uint_as_float(t[0]) + __uint_as_float(t[1]);
}
__device__ __forceinline__ unsigned pk_bf16(float lo, float hi) {
  bf16x2 v = {(__bf16)lo, (__bf16)hi};
  return __builtin_bit_cast(unsigned, v);
}
__device__ __forceinline__ f32x4 mfma32(u32x4 a, u32x4 b, f32x4 c) {
  return __builtin_amdgcn_mfma_f32_16x16x32_bf16(__builtin_bit_cast(bf16x8, a), __builtin_bit_cast(bf16x8, b), c, 0, 0, 0);
}
__device__ __forceinline__ u32x4 make_srd(const void* base, unsigned nbytes) {
  const uint64_t a = (uint64_t)base;
  return u32x4{(unsigned)a, (unsigned)(a >> 32), nbytes, 0x00020000u};
}
template <int ODD>
__device__ __forceinline__ void load8(u32x4 (&d)[8], int voff, u32x4 srd) {
  asm volatile("buffer_load_dwordx4 %0, %8, %9, 0 offen nt\n\t"
               "buffer_load_dwordx4 %1, %8, %9, 0 offen offset:%11 nt\n\t"
               "buffer_load_dwordx4 %2, %8, %9, 0 offen offset:256 nt\n\t"
               "buffer_load_dwordx4 %3, %8, %9, 0 offen offset:%12 nt\n\t"
               "buffer_load_dwordx4 %4, %8, %9, %10 offen nt\n\t"
               "buffer_load_dwordx4 %5, %8, %9, %10 offen offset:%11 nt\n\t"
               "buffer_load_dwordx4 %6, %8, %9, %10 offen offset:256 nt\n\t"
               "buffer_load_dwordx4 %7, %8, %9, %10 offen offset:%12 nt"
               : "=&v"(d[0]), "=&v"(d[1]), "=&v"(d[2]), "=&v"(d[3]), "=&v"(d[4]), "=&v"(d[5]), "=&v"(d[6]), "=&v"(d[7])
               : "v"(voff), "s"(srd), "s"(8192), "i"(ODD), "i"(256 + ODD));
  __builtin_amdgcn_sched_barrier(0);
}
__device__ __forceinline__ void wait_groups(int x, u32x4 (&r)[8]) {
  asm volatile("s_cmp_gt_u32 %8, 2\n\ts_cbranch_scc1 3f\n\t"
               "s_cmp_gt_u32 %8, 1\n\ts_cbranch_scc1 2f\n\t"
               "s_cmp_gt_u32 %8, 0\n\ts_cbranch_scc1 1f\n\t"
               "s_waitcnt vmcnt(0)\n"
               "1:\n\ts_waitcnt vmcnt(8)\n"
               "2:\n\ts_waitcnt vmcnt(16)\n"
               "3:\n\ts_waitcnt vmcnt(24)"
               : "+v"(r[0]), "+v"(r[1]), "+v"(r[2]), "+v"(r[3]), "+v"(r[4]), "+v"(r[5]), "+v"(r[6]), "+v"(r[7])
               : "s"(x) : "scc");
}
template <int T>
__device__ __forceinline__ void wait_v(int n, u32x4 (&r)[8]) {
  asm volatile("s_cmp_gt_i32 %8, %9\n\ts_cbranch_scc1 1f\n\t"
               "s_waitcnt vmcnt(0)\n"
               "1:\n\ts_waitcnt vmcnt(8)"
               : "+v"(r[0]), "+v"(r[1]), "+v"(r[2]), "+v"(r[3]), "+v"(r[4]), "+v"(r[5]), "+v"(r[6]), "+v"(r[7])
               : "s"(n), "i"(T) : "scc");
}
__device__ __forceinline__ void wg_barrier() { asm volatile("s_waitcnt lgkmcnt(0)\n\ts_barrier" ::: "memory"); }

__global__ __launch_bounds__(NTHR, 2) void attn_decode(const uint16_t* __restrict__ q, const uint16_t* __restrict__ kc,
                                                       const uint16_t* __restrict__ vc, const int* __restrict__ bt,
                                                       const int* __restrict__ lens, char* __restrict__ ws,
                                                       uint16_t* __restrict__ out) {
  __shared__ __attribute__((aligned(16))) float lds[LDS_FLOATS];
  ST(0);
  const int tid = threadIdx.x, lane = tid & 63;
  const int wave = __builtin_amdgcn_readfirstlane(tid >> 6);
  const int g = lane >> 4, c = lane & 15;
  const bool lo = c < 8;
  const int wg = blockIdx.x;
  const int hf = wg & 1, wi = (wg >> 3) + 32 * ((wg >> 1) & 3);
  const int gp = wave * 128 + wi;

  asm volatile("" ::"s"(ws), "s"(vc), "s"(kc), "s"(out));
  int len_raw, pg[NU];
  u32x4 qtmp, tag;
  const char* inbox = ws + (size_t)wg * INBOX_BYTES;
  {
    asm volatile("s_load_dword %0, %5, 0x0\n\t"
                 "s_load_dword %1, %6, 0x0\n\t"
                 "s_load_dword %2, %6, 0x1000\n\t"
                 "s_load_dword %3, %6, 0x2000\n\t"
                 "s_load_dword %4, %6, 0x3000"
                 : "=&s"(len_raw), "=&s"(pg[0]), "=&s"(pg[1]), "=&s"(pg[2]), "=&s"(pg[3])
                 : "s"(lens), "s"(bt + gp));
    const int hd = wave >> 1, ln = wave & 1;
    const int qoff = ((8 * hd + (c & 7)) * D + 64 * ln + 32 * ((c >> 3) ^ (hd & 1)) + 8 * g) * 2;
    asm volatile("global_load_dwordx4 %0, %2, %3\n\t"
                 "global_load_dwordx4 %1, %4, %5"
                 : "=&v"(qtmp), "=&v"(tag) : "v"(qoff), "s"(q), "v"((lane & 3) * 16), "s"(ws + WS_EPOCH));
    __builtin_amdgcn_sched_barrier(0);
    asm volatile("s_waitcnt lgkmcnt(0)" : "+s"(len_raw), "+s"(pg[0]), "+s"(pg[1]), "+s"(pg[2]), "+s"(pg[3]));
  }
  ST(1);
  const bool uni = len_raw <= 0;
  const int len = uni ? NB * BS : min(len_raw, NB * BS);
  const int lenk = uni ? 0 : len;
  const int n = (((len + 15) >> 4) - gp + 1023) >> 10;
  const int koff = (c >> 1) * 1024 + (c & 1) * 64 + g * 16;
  const int voff = g * 2048 + c * 16;
  const char* kbase = (const char*)kc + hf * 512;
  const char* vbase = (const char*)vc + hf * 512;

  u32x4 buf[NU][8];
  auto issue_k = [&](int i) {
    load8<128>(buf[i], koff, make_srd(kbase + (size_t)(unsigned)pg[i] * PAGE_BYTES, max(lenk - (gp + i * 1024) * 16, 0) * 1024));
  };
  auto issue_v = [&](int i) {
    load8<1024>(buf[i], voff, make_srd(vbase + (size_t)(unsigned)pg[i] * PAGE_BYTES, (len - (gp + i * 1024) * 16) * 1024));
  };
  if (0 < n) issue_k(0);
  asm volatile("s_cmp_gt_u32 %2, 0\n\ts_cbranch_scc1 1f\n\t"
               "s_waitcnt vmcnt(0)\n"
               "1:\n\ts_waitcnt vmcnt(8)"
               : "+v"(qtmp), "+v"(tag) : "s"(n) : "scc");
#pragma unroll
  for (int e = 0; e < 4; ++e) {
    const unsigned t = (tag[e] & TAG_MASK) + 4u;
    tag[e] = t >= 4u * (e == 0 ? 128 : e == 1 ? 127 : e == 2 ? 125 : 123) ? 0u : t;
  }
  asm volatile("" : "+v"(tag));
  reinterpret_cast<u32x4*>(lds + LDS_Q)[tid] = qtmp;
  wg_barrier();
  ST(2);
  if (1 < n) issue_k(1);
  if (2 < n) issue_k(2);
  u32x4 qf[4];
#pragma unroll
  for (int f = 0; f < 4; ++f) qf[f] = reinterpret_cast<const u32x4*>(lds + LDS_Q)[(4 * hf + f) * 64 + lane];

  constexpr float C2 = 0.088388347648318440f * 1.4426950408889634f;
  float sc[NU][4];
#pragma unroll
  for (int i = 0; i < NU; ++i) {
    if (i < n) {
      const int tok0 = (gp + i * 1024) * 16;
      wait_groups(i == 0 ? min(n, 3) - 1 : i == 3 ? 2 : n - 1, buf[i]);
      ST(3 + i);
      f32x4 d[4];
#pragma unroll
      for (int e = 0; e < 4; ++e) {
        d[e] = mfma32(buf[i][2 * e], qf[2 * (e & 1)], f32x4{0.f, 0.f, 0.f, 0.f});
        d[e] = mfma32(buf[i][2 * e + 1], qf[2 * (e & 1) + 1], d[e]);
      }
      __builtin_amdgcn_sched_barrier(0);
      if (i == 0) { if (3 < n) issue_k(3); issue_v(0); }
      if (i == 1) issue_v(1);
#pragma unroll
      for (int o = 0; o < 2; ++o)
#pragma unroll
        for (int a = 0; a < 2; ++a) {
          const float u = lo ? d[2 * o][2 * a] : d[2 * o + 1][2 * a];
          const float w = lo ? d[2 * o + 1][2 * a + 1] : d[2 * o][2 * a + 1];
          const float s = (u + dpp<ROR8>(w)) * C2;
          sc[i][2 * o + a] = (tok0 + 8 * o + 2 * g + a < len) ? s : -INFINITY;
        }
    } else {
#pragma unroll
      for (int k = 0; k < 4; ++k) sc[i][k] = -INFINITY;
    }
  }
  u32x2 pf[NU];
  {
    float mx = -INFINITY;
#pragma unroll
    for (int i = 0; i < NU; ++i) mx = fmaxf(mx, fmaxf(fmaxf(sc[i][0], sc[i][1]), fmaxf(sc[i][2], sc[i][3])));
    mx = xg_max(mx);
    const float ms = (mx == -INFINITY) ? 0.f : mx;
    float ls = 0.f;
#pragma unroll
    for (int i = 0; i < NU; ++i) {
#pragma unroll
      for (int k = 0; k < 4; ++k) { sc[i][k] = __builtin_amdgcn_exp2f(sc[i][k] - ms); ls += sc[i][k]; }
      pf[i] = u32x2{pk_bf16(sc[i][0], sc[i][1]), pk_bf16(sc[i][2], sc[i][3])};
    }
    ls = xg_sum(ls);
    if (g == 0) { lds[LDS_ML + c * 16 + wave] = mx; lds[LDS_ML + c * 16 + 8 + wave] = ls; }
  }
  f32x4 acc[8];
#pragma unroll
  for (int k = 0; k < 8; ++k) acc[k] = f32x4{0.f, 0.f, 0.f, 0.f};
#pragma unroll
  for (int i = 0; i < NU; ++i) {
    if (i < n) {
      if (i == 0) wait_v<1>(n, buf[0]);
      if (i == 1) wait_v<2>(n, buf[1]);
      if (i == 2) wait_v<3>(n, buf[2]);
      if (i == 3) wait_v<4>(n, buf[3]);
      if (i < 3) ST(7 + i);
      if (i + 2 < NU && i + 2 < n) issue_v(i + 2);
      const u32x4 b = {lo ? pf[i][0] : 0u, lo ? pf[i][1] : 0u, lo ? 0u : pf[i][0], lo ? 0u : pf[i][1]};
#pragma unroll
      for (int k = 0; k < 8; ++k) {
        const unsigned sel = (k & 1) ? 0x07060302u : 0x05040100u;
        const u32x4 a = {__builtin_amdgcn_perm(buf[i][1][k >> 1], buf[i][0][k >> 1], sel), __builtin_amdgcn_perm(buf[i][5][k >> 1], buf[i][4][k >> 1], sel),
                         __builtin_amdgcn_perm(buf[i][3][k >> 1], buf[i][2][k >> 1], sel), __builtin_amdgcn_perm(buf[i][7][k >> 1], buf[i][6][k >> 1], sel)};
        acc[k] = mfma32(a, b, acc[k]);
      }
    }
  }
  __builtin_amdgcn_sched_barrier(0);
  asm volatile("s_waitcnt vmcnt(0)" ::: "memory");
  __builtin_amdgcn_sched_barrier(0);
  ST(11);
  u32x4 pc;
  asm volatile("global_load_dwordx4 %0, %1, %2 sc1" : "=&v"(pc) : "v"(tid * 16), "s"(inbox));
  {
    float* rec = lds + (wave * 16 + c) * D;
#pragma unroll
    for (int k = 0; k < 8; ++k) *reinterpret_cast<f32x4*>(rec + 32 * g + 4 * (k ^ (c & 7))) = acc[k];
  }
  wg_barrier();
  ST(12);
  {
    const int h = tid >> 5, d4 = tid & 31;
    f32x4 po[NWAVE], mw[2], lw[2];
    const float* ml = lds + LDS_ML + h * 16;
    mw[0] = *reinterpret_cast<const f32x4*>(ml); mw[1] = *reinterpret_cast<const f32x4*>(ml + 4);
    lw[0] = *reinterpret_cast<const f32x4*>(ml + 8); lw[1] = *reinterpret_cast<const f32x4*>(ml + 12);
#pragma unroll
    for (int w = 0; w < NWAVE; ++w) po[w] = *reinterpret_cast<const f32x4*>(lds + (w * 16 + h) * D + 4 * (d4 ^ (h & 7)));
    __builtin_amdgcn_sched_barrier(0);
    float M = -INFINITY;
#pragma unroll
    for (int w = 0; w < NWAVE; ++w) M = fmaxf(M, mw[w >> 2][w & 3]);
    const float Ms = (M == -INFINITY) ? 0.f : M;
    float L = 0.f, wt[NWAVE];
#pragma unroll
    for (int w = 0; w < NWAVE; ++w) { wt[w] = __builtin_amdgcn_exp2f(mw[w >> 2][w & 3] - Ms); L += wt[w] * lw[w >> 2][w & 3]; }
    const float inv = L > 0.f ? __builtin_amdgcn_rcpf(L) : 0.f;
    const unsigned lse = __float_as_uint(M + __builtin_amdgcn_logf(L));
    const f32x4 o = ((wt[0] * po[0] + wt[1] * po[1]) + (wt[2] * po[2] + wt[3] * po[3])) + ((wt[4] * po[4] + wt[5] * po[5]) + (wt[6] * po[6] + wt[7] * po[7]));
    u32x4 msg;
#pragma unroll
    for (int e = 0; e < 4; ++e) msg[e] = (__float_as_uint(o[e] * inv) & DATA_MASK) | tag[e] | ((lse >> (8 * (d4 & 3) + 2 * e)) & 3u);
    const int moff = ((16 * hf + h) * 8 + (d4 >> 2)) * INBOX_BYTES + wi * 64 + (d4 & 3) * 16;
    asm volatile("global_store_dwordx4 %0, %1, %2 sc1\n\ts_nop 1" :: "v"(moff), "v"(msg), "s"(ws) : "memory");
    asm volatile("s_waitcnt vmcnt(1)" : "+v"(pc));
  }
  ST(13);
  for (unsigned spin = 0; spin < SPIN_CAP; ++spin) {
    unsigned x = 0;
#pragma unroll
    for (int e = 0; e < 4; ++e) x |= pc[e] ^ tag[e];
    x &= TAG_MASK;
    x |= __builtin_amdgcn_mov_dpp(x, QX1, 0xf, 0xf, true);
    x |= __builtin_amdgcn_mov_dpp(x, QX2, 0xf, 0xf, true);
    const unsigned long long miss = __builtin_amdgcn_ballot_w64(x != 0u);
    if (miss == 0ull) break;
    if (__builtin_popcountll(miss) > 16) __builtin_amdgcn_s_sleep(16);
    asm volatile("s_mov_b64 exec, %2\n\t"
                 "global_load_dwordx4 %0, %1, %3 sc1\n\t"
                 "s_mov_b64 exec, -1\n\t"
                 "s_waitcnt vmcnt(0)"
                 : "+v"(pc) : "v"(tid * 16), "s"(miss), "s"(inbox) : "memory");
  }
  if (wg == 128 && wave >= 6) {
    for (unsigned spin = 0; spin < SPIN_CAP; ++spin) {
      u32x4 w;
      asm volatile("global_load_dwordx4 %0, %1, %2 sc1\n\ts_waitcnt vmcnt(0)" : "=&v"(w) : "v"((tid - 384) * 64 + (lane & 3) * 16), "s"(ws) : "memory");
      unsigned x = 0;
#pragma unroll
      for (int e = 0; e < 4; ++e) x |= w[e] ^ tag[e];
      if (__builtin_amdgcn_ballot_w64((x & TAG_MASK) != 0u) == 0ull) break;
    }
  }
  ST(14);
  {
    float* mrg = lds + LDS_Q;
    unsigned b = ((pc[0] & 3u) | ((pc[1] & 3u) << 2) | ((pc[2] & 3u) << 4) | ((pc[3] & 3u) << 6)) << (8 * (lane & 3));
    b |= __builtin_amdgcn_mov_dpp(b, QX1, 0xf, 0xf, true);
    b |= __builtin_amdgcn_mov_dpp(b, QX2, 0xf, 0xf, true);
    const float ls = __uint_as_float(b);
    float M = ls;
    M = fmaxf(M, dpp<ROR4>(M)); M = fmaxf(M, dpp<ROR8>(M)); M = xg_max(M);
    const float Ms = (M == -INFINITY) ? 0.f : M;
    const float w0 = __builtin_amdgcn_exp2f(ls - Ms);
    float v[5];
#pragma unroll
    for (int e = 0; e < 4; ++e) v[e] = w0 * __uint_as_float(pc[e] & DATA_MASK);
    v[4] = w0;
#pragma unroll
    for (int e = 0; e < 5; ++e) { v[e] += dpp<ROR4>(v[e]); v[e] += dpp<ROR8>(v[e]); v[e] = xg_sum(v[e]); }
    if (lane < 4) {
      *reinterpret_cast<f32x4*>(mrg + (wave * 4 + lane) * 8) = f32x4{v[0], v[1], v[2], v[3]};
      *reinterpret_cast<f32x2*>(mrg + (wave * 4 + lane) * 8 + 4) = f32x2{v[4], M};
    }
    wg_barrier();
    if (wave == 0) {
      const int r = lane & 31;
      const f32x4 o = *reinterpret_cast<const f32x4*>(mrg + r * 8);
      const f32x2 wm = *reinterpret_cast<const f32x2*>(mrg + r * 8 + 4);
      float MM = wm[1];
      MM = fmaxf(MM, dpp<ROR4>(MM)); MM = fmaxf(MM, dpp<ROR8>(MM)); MM = x16_max(MM);
      const float wt = __builtin_amdgcn_exp2f(wm[1] - MM);
      float u[5] = {o[0] * wt, o[1] * wt, o[2] * wt, o[3] * wt, wm[0] * wt};
#pragma unroll
      for (int e = 0; e < 5; ++e) { u[e] += dpp<ROR4>(u[e]); u[e] += dpp<ROR8>(u[e]); u[e] = x16_sum(u[e]); }
      if (lane < 4) {
        const float inv = __builtin_amdgcn_rcpf(u[4]);
        uint16_t* o16 = out + (wg >> 3) * D + 32 * ((wg & 7) >> 1) + 4 * (wg & 1) + lane;
        const unsigned p01 = pk_bf16(u[0] * inv, u[1] * inv), p23 = pk_bf16(u[2] * inv, u[3] * inv);
        o16[0] = (uint16_t)p01; o16[8] = (uint16_t)(p01 >> 16); o16[16] = (uint16_t)p23; o16[24] = (uint16_t)(p23 >> 16);
        if (wg == 128) asm volatile("global_store_dwordx4 %0, %1, %2 sc1\n\ts_nop 1" :: "v"(lane * 16), "v"(tag), "s"(ws + WS_EPOCH) : "memory");
      }
    }
  }
  ST(10);
}
}

extern "C" void kernel_launch(void* const* d_in, const int* in_sizes, int n_in, void* d_out, int out_size, void* d_ws,
                              size_t ws_size, hipStream_t stream) {
  attn_decode<<<dim3(NWG), dim3(NTHR), 0, stream>>>((const uint16_t*)d_in[0], (const uint16_t*)d_in[1], (const uint16_t*)d_in[2],
                                                    (const int*)d_in[3], (const int*)d_in[4], (char*)d_ws, (uint16_t*)d_out);
}
```

```cpp
#include <hip/hip_runtime.h>
#include <hip/hip_bf16.h>
#include <stdint.h>

#define ST(i) do {} while (0)

#define PRM 2
#define PRIO(x) __builtin_amdgcn_s_setprio(x)
namespace {
constexpr int HKV = 4, D = 128, BS = 16;
constexpr int NB = 4096;
constexpr int NWG = 256, NWAVE = 8, NTHR = NWAVE * 64;
constexpr int NU = NB / (NWG / 2 * NWAVE);
constexpr int PAGE_BYTES = BS * HKV * D * 2;
static_assert(NU == 4, "written for 4 pages per wave");
constexpr int LDS_Q = NWAVE * 16 * D;
constexpr int LDS_ML = LDS_Q + 2048;
constexpr int LDS_CNT = LDS_ML + 16 * 16;
constexpr int LDS_FLOATS = LDS_CNT + 4;
constexpr int INBOX_BYTES = 128 * 64;
constexpr int WS_EPOCH = NWG * INBOX_BYTES;
constexpr unsigned TAG_MASK = 0x1fcu, DATA_MASK = 0xfffffe00u;
constexpr unsigned SPIN_CAP = 1u << 15;

using bf16x8 = __attribute__((ext_vector_type(8))) __bf16;
using bf16x2 = __attribute__((ext_vector_type(2))) __bf16;
using f32x4 = __attribute__((ext_vector_type(4))) float;
using f32x2 = __attribute__((ext_vector_type(2))) float;
using u32x4 = __attribute__((ext_vector_type(4))) unsigned;
using u32x2 = __attribute__((ext_vector_type(2))) unsigned;

template <int CTRL> __device__ __forceinline__ float dpp(float x) {
  return __builtin_bit_cast(float, __builtin_amdgcn_mov_dpp(__builtin_bit_cast(int, x), CTRL, 0xf, 0xf, true));
}
constexpr int ROR4 = 0x124, ROR8 = 0x128, QX1 = 0xB1, QX2 = 0x4E;
__device__ __forceinline__ float x16_sum(float x) {
  auto s = __builtin_amdgcn_permlane16_swap(__float_as_uint(x), __float_as_uint(x), false, false);
  return __uint_as_float(s[0]) + __uint_as_float(s[1]);
}
__device__ __forceinline__ float x16_max(float x) {
  auto s = __builtin_amdgcn_permlane16_swap(__float_as_uint(x), __float_as_uint(x), false, false);
  return fmaxf(__uint_as_float(s[0]), __uint_as_float(s[1]));
}
__device__ __forceinline__ float xg_max(float x) {
  x = x16_max(x);
  auto t = __builtin_amdgcn_permlane32_swap(__float_as_uint(x), __float_as_uint(x), false, false);
  return fmaxf(__uint_as_float(t[0]), __uint_as_float(t[1]));
}
__device__ __forceinline__ float xg_sum(float x) {
  x = x16_sum(x);
  auto t = __builtin_amdgcn_permlane32_swap(__float_as_uint(x), __float_as_uint(x), false, false);
  return __uint_as_float(t[0]) + __uint_as_float(t[1]);
}
__device__ __forceinline__ unsigned pk_bf16(float lo, float hi) {
  bf16x2 v = {(__bf16)lo, (__bf16)hi};
  return __builtin_bit_cast(unsigned, v);
}
__device__ __forceinline__ f32x4 mfma32(u32x4 a, u32x4 b, f32x4 c) {
  return __builtin_amdgcn_mfma_f32_16x16x32_bf16(__builtin_bit_cast(bf16x8, a), __builtin_bit_cast(bf16x8, b), c, 0, 0, 0);
}
__device__ __forceinline__ u32x4 make_srd(const void* base, unsigned nbytes) {
  const uint64_t a = (uint64_t)base;
  return u32x4{(unsigned)a, (unsigned)(a >> 32), nbytes, 0x00020000u};
}
template <int ODD>
__device__ __forceinline__ void load8(u32x4 (&d)[8], int voff, u32x4 srd) {
  asm volatile("buffer_load_dwordx4 %0, %8, %9, 0 offen nt\n\t"
               "buffer_load_dwordx4 %1, %8, %9, 0 offen offset:%11 nt\n\t"
               "buffer_load_dwordx4 %2, %8, %9, 0 offen offset:256 nt\n\t"
               "buffer_load_dwordx4 %3, %8, %9, 0 offen offset:%12 nt\n\t"
               "buffer_load_dwordx4 %4, %8, %9, %10 offen nt\n\t"
               "buffer_load_dwordx4 %5, %8, %9, %10 offen offset:%11 nt\n\t"
               "buffer_load_dwordx4 %6, %8, %9, %10 offen offset:256 nt\n\t"
               "buffer_load_dwordx4 %7, %8, %9, %10 offen offset:%12 nt"
               : "=&v"(d[0]), "=&v"(d[1]), "=&v"(d[2]), "=&v"(d[3]), "=&v"(d[4]), "=&v"(d[5]), "=&v"(d[6]), "=&v"(d[7])
               : "v"(voff), "s"(srd), "s"(8192), "i"(ODD), "i"(256 + ODD));
  __builtin_amdgcn_sched_barrier(0);
}
__device__ __forceinline__ void wait_groups(int x, u32x4 (&r)[8]) {
  asm volatile("s_cmp_gt_u32 %8, 2\n\ts_cbranch_scc1 3f\n\t"
               "s_cmp_gt_u32 %8, 1\n\ts_cbranch_scc1 2f\n\t"
               "s_cmp_gt_u32 %8, 0\n\ts_cbranch_scc1 1f\n\t"
               "s_waitcnt vmcnt(0)\n"
               "1:\n\ts_waitcnt vmcnt(8)\n"
               "2:\n\ts_waitcnt vmcnt(16)\n"
               "3:\n\ts_waitcnt vmcnt(24)"
               : "+v"(r[0]), "+v"(r[1]), "+v"(r[2]), "+v"(r[3]), "+v"(r[4]), "+v"(r[5]), "+v"(r[6]), "+v"(r[7])
               : "s"(x) : "scc");
}
template <int N>
__device__ __forceinline__ void wait_v(u32x4 (&r)[8]) {
  asm volatile("s_waitcnt vmcnt(%8)"
               : "+v"(r[0]), "+v"(r[1]), "+v"(r[2]), "+v"(r[3]), "+v"(r[4]), "+v"(r[5]), "+v"(r[6]), "+v"(r[7]) : "i"(N));
}
__device__ __forceinline__ void wg_barrier() { asm volatile("s_waitcnt lgkmcnt(0)\n\ts_barrier" ::: "memory"); }

__global__ __launch_bounds__(NTHR, 2) void attn_decode(const uint16_t* __restrict__ q, const uint16_t* __restrict__ kc,
                                                       const uint16_t* __restrict__ vc, const int* __restrict__ bt,
                                                       const int* __restrict__ lens, char* __restrict__ ws,
                                                       uint16_t* __restrict__ out) {
  __shared__ __attribute__((aligned(16))) float lds[LDS_FLOATS];
  ST(0);
  const int tid = threadIdx.x, lane = tid & 63;
  const int wave = __builtin_amdgcn_readfirstlane(tid >> 6);
  const int g = lane >> 4, c = lane & 15;
  const bool lo = c < 8;
  const int wg = blockIdx.x;
  const int hf = wg & 1, wi = (wg >> 3) + 32 * ((wg >> 1) & 3);
  const int gp = wave * 128 + wi;

  if (PRM >= 1) PRIO(3);
  asm volatile("" ::"s"(ws), "s"(vc), "s"(kc), "s"(out));
  int len_raw, pg[NU];
  u32x4 qtmp, tag;
  const char* inbox = ws + (size_t)wg * INBOX_BYTES;
  {
    asm volatile("s_load_dword %0, %5, 0x0\n\t"
                 "s_load_dword %1, %6, 0x0\n\t"
                 "s_load_dword %2, %6, 0x1000\n\t"
                 "s_load_dword %3, %6, 0x2000\n\t"
                 "s_load_dword %4, %6, 0x3000"
                 : "=&s"(len_raw), "=&s"(pg[0]), "=&s"(pg[1]), "=&s"(pg[2]), "=&s"(pg[3])
                 : "s"(lens), "s"(bt + gp));
    const int hd = wave >> 1, ln = wave & 1;
    const int qoff = ((8 * hd + (c & 7)) * D + 64 * ln + 32 * ((c >> 3) ^ (hd & 1)) + 8 * g) * 2;
    asm volatile("global_load_dwordx4 %0, %2, %3\n\t"
                 "global_load_dwordx4 %1, %4, %5"
                 : "=&v"(qtmp), "=&v"(tag) : "v"(qoff), "s"(q), "v"((lane & 3) * 16), "s"(ws + WS_EPOCH));
    __builtin_amdgcn_sched_barrier(0);
    asm volatile("s_waitcnt lgkmcnt(0)" : "+s"(len_raw), "+s"(pg[0]), "+s"(pg[1]), "+s"(pg[2]), "+s"(pg[3]));
  }
  ST(1);
  const bool uni = len_raw <= 0;
  const int len = uni ? NB * BS : min(len_raw, NB * BS);
  const int lenk = uni ? 0 : len;
  const int n = (((len + 15) >> 4) - gp + 1023) >> 10;
  const int koff = (c >> 1) * 1024 + (c & 1) * 64 + g * 16;
  const int voff = g * 2048 + c * 16;
  const char* kbase = (const char*)kc + hf * 512;
  const char* vbase = (const char*)vc + hf * 512;

  u32x4 buf[NU][8];
  auto issue_k = [&](int i) {
    load8<128>(buf[i], koff, make_srd(kbase + (size_t)(unsigned)pg[i] * PAGE_BYTES, max(lenk - (gp + i * 1024) * 16, 0) * 1024));
  };
  auto issue_v = [&](int i) {
    load8<1024>(buf[i], voff, make_srd(vbase + (size_t)(unsigned)pg[i] * PAGE_BYTES, (len - (gp + i * 1024) * 16) * 1024));
  };
  if (0 < n) issue_k(0);
  asm volatile("s_cmp_gt_u32 %2, 0\n\ts_cbranch_scc1 1f\n\t"
               "s_waitcnt vmcnt(0)\n"
               "1:\n\ts_waitcnt vmcnt(8)"
               : "+v"(qtmp), "+v"(tag) : "s"(n) : "scc");
#pragma unroll
  for (int e = 0; e < 4; ++e) {
    const unsigned t = (tag[e] & TAG_MASK) + 4u;
    tag[e] = t >= 4u * (e == 0 ? 128 : e == 1 ? 127 : e == 2 ? 125 : 123) ? 0u : t;
  }
  asm volatile("" : "+v"(tag));
  reinterpret_cast<u32x4*>(lds + LDS_Q)[tid] = qtmp;
  if (tid == 0) reinterpret_cast<int*>(lds)[LDS_CNT] = 0;
  wg_barrier();
  ST(2);
  if (1 < n) issue_k(1);
  if (2 < n) issue_k(2);
  u32x4 qf[4];
#pragma unroll
  for (int f = 0; f < 4; ++f) qf[f] = reinterpret_cast<const u32x4*>(lds + LDS_Q)[(4 * hf + f) * 64 + lane];

  constexpr float C2 = 0.088388347648318440f * 1.4426950408889634f;
  float sc[NU][4];
#pragma unroll
  for (int i = 0; i < NU; ++i) {
    if (i < n) {
      const int tok0 = (gp + i * 1024) * 16;
      wait_groups(i == 0 ? min(n, 3) - 1 : i == 3 ? 2 : n - 1, buf[i]);
      ST(3 + i);
      f32x4 d[4];
#pragma unroll
      for (int e = 0; e < 4; ++e) {
        d[e] = mfma32(buf[i][2 * e], qf[2 * (e & 1)], f32x4{0.f, 0.f, 0.f, 0.f});
        d[e] = mfma32(buf[i][2 * e + 1], qf[2 * (e & 1) + 1], d[e]);
      }
      __builtin_amdgcn_sched_barrier(0);
      if (i == 0) { if (3 < n) issue_k(3); if (PRM == 1) { if (3 >= n) PRIO(2); } if (PRM == 2) PRIO(0); issue_v(0); }
      if (i == 1) { if (PRM == 1) { if (3 < n) PRIO(2); else PRIO(1); } if (PRM == 3) PRIO(0); issue_v(1); }
#pragma unroll
      for (int o = 0; o < 2; ++o)
#pragma unroll
        for (int a = 0; a < 2; ++a) {
          const float u = lo ? d[2 * o][2 * a] : d[2 * o + 1][2 * a];
          const float w = lo ? d[2 * o + 1][2 * a + 1] : d[2 * o][2 * a + 1];
          const float s = (u + dpp<ROR8>(w)) * C2;
          sc[i][2 * o + a] = (tok0 + 8 * o + 2 * g + a < len) ? s : -INFINITY;
        }
    } else {
#pragma unroll
      for (int k = 0; k < 4; ++k) sc[i][k] = -INFINITY;
    }
  }
  u32x2 pf[NU - 1];
  float mx = -INFINITY, pl[4];
  {
#pragma unroll
    for (int i = 0; i < NU; ++i) mx = fmaxf(mx, fmaxf(fmaxf(sc[i][0], sc[i][1]), fmaxf(sc[i][2], sc[i][3])));
    mx = xg_max(mx);
    const float ms = (mx == -INFINITY) ? 0.f : mx;
    float ls = 0.f;
#pragma unroll
    for (int i = 0; i < NU; ++i) {
#pragma unroll
      for (int k = 0; k < 4; ++k) { sc[i][k] = __builtin_amdgcn_exp2f(sc[i][k] - ms); ls += sc[i][k]; }
      if (i < NU - 1) pf[i] = u32x2{pk_bf16(sc[i][0], sc[i][1]), pk_bf16(sc[i][2], sc[i][3])};
    }
#pragma unroll
    for (int k = 0; k < 4; ++k) pl[k] = n > 3 ? sc[3][k] : n > 2 ? sc[2][k] : n > 1 ? sc[1][k] : sc[0][k];
    ls = xg_sum(ls);
    if (g == 0) { lds[LDS_ML + c * 16 + wave] = mx; lds[LDS_ML + c * 16 + 8 + wave] = ls; }
    asm volatile("s_waitcnt lgkmcnt(0)\n\t"
                 "s_mov_b64 exec, 1\n\t"
                 "ds_add_u32 %0, %1\n\t"
                 "s_mov_b64 exec, -1"
                 :: "v"((int)(uintptr_t)(__attribute__((address_space(3))) float*)(lds + LDS_CNT)), "v"(1) : "memory");
  }
  if (PRM == 1) { if (3 < n) PRIO(1); else PRIO(0); }
  f32x4 acc[8];
#pragma unroll
  for (int k = 0; k < 8; ++k) acc[k] = f32x4{0.f, 0.f, 0.f, 0.f};
  auto pv = [&](u32x4 (&vb)[8], u32x2 p) {
    const u32x4 b = {lo ? p[0] : 0u, lo ? p[1] : 0u, lo ? 0u : p[0], lo ? 0u : p[1]};
#pragma unroll
    for (int k = 0; k < 8; ++k) {
      const unsigned sel = (k & 1) ? 0x07060302u : 0x05040100u;
      const u32x4 a = {__builtin_amdgcn_perm(vb[1][k >> 1], vb[0][k >> 1], sel), __builtin_amdgcn_perm(vb[5][k >> 1], vb[4][k >> 1], sel),
                       __builtin_amdgcn_perm(vb[3][k >> 1], vb[2][k >> 1], sel), __builtin_amdgcn_perm(vb[7][k >> 1], vb[6][k >> 1], sel)};
      acc[k] = mfma32(a, b, acc[k]);
    }
  };
#pragma unroll
  for (int i = 0; i < NU - 1; ++i) {
    if (i + 1 < n) {
      wait_v<8>(buf[i]);
      ST(7 + i);
      if (i + 2 < NU && i + 2 < n) issue_v(i + 2);
      pv(buf[i], pf[i]);
    }
  }
  u32x4 pc;
  asm volatile("global_load_dwordx4 %0, %1, %2 sc1" : "=&v"(pc) : "v"(tid * 16), "s"(inbox));
  const int h = tid >> 5, d4 = tid & 31;
  unsigned low[4];
  u32x2 plast;
  {
    for (unsigned spin = 0; spin < (1u << 20); ++spin) {
      if (__builtin_amdgcn_readfirstlane(__hip_atomic_load(reinterpret_cast<int*>(lds) + LDS_CNT, __ATOMIC_RELAXED, __HIP_MEMORY_SCOPE_WORKGROUP)) >= NWAVE) break;
      __builtin_amdgcn_s_sleep(2);
    }
    asm volatile("" ::: "memory");
    float f = 0.f;
#pragma unroll
    for (int r = 0; r < 2; ++r) {
      const float* ml = lds + LDS_ML + (r ? h : c) * 16;
      const f32x4 m0 = *reinterpret_cast<const f32x4*>(ml), m1 = *reinterpret_cast<const f32x4*>(ml + 4);
      const f32x4 l0 = *reinterpret_cast<const f32x4*>(ml + 8), l1 = *reinterpret_cast<const f32x4*>(ml + 12);
      const float M = fmaxf(fmaxf(fmaxf(m0[0], m0[1]), fmaxf(m0[2], m0[3])), fmaxf(fmaxf(m1[0], m1[1]), fmaxf(m1[2], m1[3])));
      const float Ms = (M == -INFINITY) ? 0.f : M;
      float L = 0.f;
#pragma unroll
      for (int w = 0; w < 4; ++w) L += __builtin_amdgcn_exp2f(m0[w] - Ms) * l0[w];
#pragma unroll
      for (int w = 0; w < 4; ++w) L += __builtin_amdgcn_exp2f(m1[w] - Ms) * l1[w];
      if (r == 0) {
        f = L > 0.f ? __builtin_amdgcn_exp2f(mx - Ms) * __builtin_amdgcn_rcpf(L) : 0.f;
      } else {
        const unsigned lse = __float_as_uint(M + __builtin_amdgcn_logf(L));
#pragma unroll
        for (int e = 0; e < 4; ++e) low[e] = tag[e] | ((lse >> (8 * (d4 & 3) + 2 * e)) & 3u);
      }
    }
#pragma unroll
    for (int k = 0; k < 8; ++k) acc[k] *= f;
    plast = u32x2{pk_bf16(pl[0] * f, pl[1] * f), pk_bf16(pl[2] * f, pl[3] * f)};
    asm volatile("" : "+v"(plast), "+v"(low[0]), "+v"(low[1]), "+v"(low[2]), "+v"(low[3]));
  }
#pragma unroll
  for (int i = 0; i < NU; ++i) {
    if (i + 1 == n) {
      wait_v<1>(buf[i]);
      pv(buf[i], plast);
    }
  }
  __builtin_amdgcn_sched_barrier(0);
  asm volatile("s_waitcnt vmcnt(1)" ::: "memory");
  __builtin_amdgcn_sched_barrier(0);
  ST(11);
  {
    float* rec = lds + (wave * 16 + c) * D;
#pragma unroll
    for (int k = 0; k < 8; ++k) *reinterpret_cast<f32x4*>(rec + 32 * g + 4 * (k ^ (c & 7))) = acc[k];
  }
  const int moff = ((16 * hf + h) * 8 + (d4 >> 2)) * INBOX_BYTES + wi * 64 + (d4 & 3) * 16;
  wg_barrier();
  ST(12);
  {
    f32x4 po[NWAVE];
#pragma unroll
    for (int w = 0; w < NWAVE; ++w) po[w] = *reinterpret_cast<const f32x4*>(lds + (w * 16 + h) * D + 4 * (d4 ^ (h & 7)));
    const f32x4 o = ((po[0] + po[1]) + (po[2] + po[3])) + ((po[4] + po[5]) + (po[6] + po[7]));
    u32x4 msg;
#pragma unroll
    for (int e = 0; e < 4; ++e) msg[e] = (__float_as_uint(o[e]) & DATA_MASK) | low[e];
    asm volatile("global_store_dwordx4 %0, %1, %2 sc1\n\ts_nop 1" :: "v"(moff), "v"(msg), "s"(ws) : "memory");
    asm volatile("s_waitcnt vmcnt(1)" : "+v"(pc));
  }
  ST(13);
  for (unsigned spin = 0; spin < SPIN_CAP; ++spin) {
    unsigned x = 0;
#pragma unroll
    for (int e = 0; e < 4; ++e) x |= pc[e] ^ tag[e];
    x &= TAG_MASK;
    x |= __builtin_amdgcn_mov_dpp(x, QX1, 0xf, 0xf, true);
    x |= __builtin_amdgcn_mov_dpp(x, QX2, 0xf, 0xf, true);
    const unsigned long long miss = __builtin_amdgcn_ballot_w64(x != 0u);
    if (miss == 0ull) break;
    if (__builtin_popcountll(miss) > 16) __builtin_amdgcn_s_sleep(16);
    asm volatile("s_mov_b64 exec, %2\n\t"
                 "global_load_dwordx4 %0, %1, %3 sc1\n\t"
                 "s_mov_b64 exec, -1\n\t"
                 "s_waitcnt vmcnt(0)"
                 : "+v"(pc) : "v"(tid * 16), "s"(miss), "s"(inbox) : "memory");
  }
  if (wg == 128 && wave >= 6) {
    for (unsigned spin = 0; spin < SPIN_CAP; ++spin) {
      u32x4 w;
      asm volatile("global_load_dwordx4 %0, %1, %2 sc1\n\ts_waitcnt vmcnt(0)" : "=&v"(w) : "v"((tid - 384) * 64 + (lane & 3) * 16), "s"(ws) : "memory");
      unsigned x = 0;
#pragma unroll
      for (int e = 0; e < 4; ++e) x |= w[e] ^ tag[e];
      if (__builtin_amdgcn_ballot_w64((x & TAG_MASK) != 0u) == 0ull) break;
    }
  }
  ST(14);
  {
    float* mrg = lds + LDS_Q;
    unsigned b = ((pc[0] & 3u) | ((pc[1] & 3u) << 2) | ((pc[2] & 3u) << 4) | ((pc[3] & 3u) << 6)) << (8 * (lane & 3));
    b |= __builtin_amdgcn_mov_dpp(b, QX1, 0xf, 0xf, true);
    b |= __builtin_amdgcn_mov_dpp(b, QX2, 0xf, 0xf, true);
    const float ls = __uint_as_float(b);
    float M = ls;
    M = fmaxf(M, dpp<ROR4>(M)); M = fmaxf(M, dpp<ROR8>(M)); M = xg_max(M);
    const float Ms = (M == -INFINITY) ? 0.f : M;
    const float w0 = __builtin_amdgcn_exp2f(ls - Ms);
    float v[5];
#pragma unroll
    for (int e = 0; e < 4; ++e) v[e] = w0 * __uint_as_float(pc[e] & DATA_MASK);
    v[4] = w0;
#pragma unroll
    for (int e = 0; e < 5; ++e) { v[e] += dpp<ROR4>(v[e]); v[e] += dpp<ROR8>(v[e]); v[e] = xg_sum(v[e]); }
    if (lane < 4) {
      *reinterpret_cast<f32x4*>(mrg + (wave * 4 + lane) * 8) = f32x4{v[0], v[1], v[2], v[3]};
      *reinterpret_cast<f32x2*>(mrg + (wave * 4 + lane) * 8 + 4) = f32x2{v[4], M};
    }
    wg_barrier();
    if (wave == 0) {
      const int r = lane & 31;
      const f32x4 o = *reinterpret_cast<const f32x4*>(mrg + r * 8);
      const f32x2 wm = *reinterpret_cast<const f32x2*>(mrg + r * 8 + 4);
      float MM = wm[1];
      MM = fmaxf(MM, dpp<ROR4>(MM)); MM = fmaxf(MM, dpp<ROR8>(MM)); MM = x16_max(MM);
      const float wt = __builtin_amdgcn_exp2f(wm[1] - MM);
      float u[5] = {o[0] * wt, o[1] * wt, o[2] * wt, o[3] * wt, wm[0] * wt};
#pragma unroll
      for (int e = 0; e < 5; ++e) { u[e] += dpp<ROR4>(u[e]); u[e] += dpp<ROR8>(u[e]); u[e] = x16_sum(u[e]); }
      if (lane < 4) {
        const float inv = __builtin_amdgcn_rcpf(u[4]);
        uint16_t* o16 = out + (wg >> 3) * D + 32 * ((wg & 7) >> 1) + 4 * (wg & 1) + lane;
        const unsigned p01 = pk_bf16(u[0] * inv, u[1] * inv), p23 = pk_bf16(u[2] * inv, u[3] * inv);
        o16[0] = (uint16_t)p01; o16[8] = (uint16_t)(p01 >> 16); o16[16] = (uint16_t)p23; o16[24] = (uint16_t)(p23 >> 16);
        if (wg == 128) asm volatile("global_store_dwordx4 %0, %1, %2 sc1\n\ts_nop 1" :: "v"(lane * 16), "v"(tag), "s"(ws + WS_EPOCH) : "memory");
      }
    }
  }
  ST(10);
}
}

extern "C" void kernel_launch(void* const* d_in, const int* in_sizes, int n_in, void* d_out, int out_size, void* d_ws,
                              size_t ws_size, hipStream_t stream) {
  attn_decode<<<dim3(NWG), dim3(NTHR), 0, stream>>>((const uint16_t*)d_in[0], (const uint16_t*)d_in[1], (const uint16_t*)d_in[2],
                                                    (const int*)d_in[3], (const int*)d_in[4], (char*)d_ws, (uint16_t*)d_out);
}
```

```cpp
#include <hip/hip_runtime.h>
#include <hip/hip_bf16.h>
#include <stdint.h>

namespace {
constexpr int HKV = 4, D = 128, BS = 16;
constexpr int NB = 4096;
constexpr int NWG = 256, NWAVE = 8, NTHR = NWAVE * 64;
constexpr int NU = NB / (NWG / 2 * NWAVE);
constexpr int PAGE_BYTES = BS * HKV * D * 2;
static_assert(NU == 4, "written for 4 pages per wave");
constexpr int LDS_Q = NWAVE * 16 * D;
constexpr int LDS_ML = LDS_Q + 2048;
constexpr int LDS_CNT = LDS_ML + 16 * 16;
constexpr int LDS_FLOATS = LDS_CNT + 4;
constexpr int INBOX_BYTES = 128 * 64;
constexpr int WS_EPOCH = NWG * INBOX_BYTES;
constexpr unsigned TAG_MASK = 0x1fcu, DATA_MASK = 0xfffffe00u;
constexpr unsigned SPIN_CAP = 1u << 15;

using bf16x8 = __attribute__((ext_vector_type(8))) __bf16;
using bf16x2 = __attribute__((ext_vector_type(2))) __bf16;
using f32x4 = __attribute__((ext_vector_type(4))) float;
using f32x2 = __attribute__((ext_vector_type(2))) float;
using u32x4 = __attribute__((ext_vector_type(4))) unsigned;
using u32x2 = __attribute__((ext_vector_type(2))) unsigned;

template <int CTRL> __device__ __forceinline__ float dpp(float x) {
  return __builtin_bit_cast(float, __builtin_amdgcn_mov_dpp(__builtin_bit_cast(int, x), CTRL, 0xf, 0xf, true));
}
constexpr int ROR4 = 0x124, ROR8 = 0x128, QX1 = 0xB1, QX2 = 0x4E;
__device__ __forceinline__ float x16_sum(float x) {
  auto s = __builtin_amdgcn_permlane16_swap(__float_as_uint(x), __float_as_uint(x), false, false);
  return __uint_as_float(s[0]) + __uint_as_float(s[1]);
}
__device__ __forceinline__ float x16_max(float x) {
  auto s = __builtin_amdgcn_permlane16_swap(__float_as_uint(x), __float_as_uint(x), false, false);
  return fmaxf(__uint_as_float(s[0]), __uint_as_float(s[1]));
}
__device__ __forceinline__ float xg_max(float x) {
  x = x16_max(x);
  auto t = __builtin_amdgcn_permlane32_swap(__float_as_uint(x), __float_as_uint(x), false, false);
  return fmaxf(__uint_as_float(t[0]), __uint_as_float(t[1]));
}
__device__ __forceinline__ float xg_sum(float x) {
  x = x16_sum(x);
  auto t = __builtin_amdgcn_permlane32_swap(__float_as_uint(x), __float_as_uint(x), false, false);
  return __uint_as_float(t[0]) + __uint_as_float(t[1]);
}
__device__ __forceinline__ unsigned pk_bf16(float lo, float hi) {
  bf16x2 v = {(__bf16)lo, (__bf16)hi};
  return __builtin_bit_cast(unsigned, v);
}
__device__ __forceinline__ f32x4 mfma32(u32x4 a, u32x4 b, f32x4 c) {
  return __builtin_amdgcn_mfma_f32_16x16x32_bf16(__builtin_bit_cast(bf16x8, a), __builtin_bit_cast(bf16x8, b), c, 0, 0, 0);
}
__device__ __forceinline__ u32x4 make_srd(const void* base, unsigned nbytes) {
  const uint64_t a = (uint64_t)base;
  return u32x4{(unsigned)a, (unsigned)(a >> 32), nbytes, 0x00020000u};
}
template <int ODD>
__device__ __forceinline__ void load8(u32x4 (&d)[8], int voff, u32x4 srd) {
  asm volatile("buffer_load_dwordx4 %0, %8, %9, 0 offen nt\n\t"
               "buffer_load_dwordx4 %1, %8, %9, 0 offen offset:%11 nt\n\t"
               "buffer_load_dwordx4 %2, %8, %9, 0 offen offset:256 nt\n\t"
               "buffer_load_dwordx4 %3, %8, %9, 0 offen offset:%12 nt\n\t"
               "buffer_load_dwordx4 %4, %8, %9, %10 offen nt\n\t"
               "buffer_load_dwordx4 %5, %8, %9, %10 offen offset:%11 nt\n\t"
               "buffer_load_dwordx4 %6, %8, %9, %10 offen offset:256 nt\n\t"
               "buffer_load_dwordx4 %7, %8, %9, %10 offen offset:%12 nt"
               : "=&v"(d[0]), "=&v"(d[1]), "=&v"(d[2]), "=&v"(d[3]), "=&v"(d[4]), "=&v"(d[5]), "=&v"(d[6]), "=&v"(d[7])
               : "v"(voff), "s"(srd), "s"(8192), "i"(ODD), "i"(256 + ODD));
  __builtin_amdgcn_sched_barrier(0);
}
__device__ __forceinline__ void wait_groups(int x, u32x4 (&r)[8]) {
  asm volatile("s_cmp_gt_u32 %8, 2\n\ts_cbranch_scc1 3f\n\t"
               "s_cmp_gt_u32 %8, 1\n\ts_cbranch_scc1 2f\n\t"
               "s_cmp_gt_u32 %8, 0\n\ts_cbranch_scc1 1f\n\t"
               "s_waitcnt vmcnt(0)\n"
               "1:\n\ts_waitcnt vmcnt(8)\n"
               "2:\n\ts_waitcnt vmcnt(16)\n"
               "3:\n\ts_waitcnt vmcnt(24)"
               : "+v"(r[0]), "+v"(r[1]), "+v"(r[2]), "+v"(r[3]), "+v"(r[4]), "+v"(r[5]), "+v"(r[6]), "+v"(r[7])
               : "s"(x) : "scc");
}
template <int N>
__device__ __forceinline__ void wait_v(u32x4 (&r)[8]) {
  asm volatile("s_waitcnt vmcnt(%8)"
               : "+v"(r[0]), "+v"(r[1]), "+v"(r[2]), "+v"(r[3]), "+v"(r[4]), "+v"(r[5]), "+v"(r[6]), "+v"(r[7]) : "i"(N));
}
__device__ __forceinline__ void wg_barrier() { asm volatile("s_waitcnt lgkmcnt(0)\n\ts_barrier" ::: "memory"); }

__global__ __launch_bounds__(NTHR, 2) void attn_decode(const uint16_t* __restrict__ q, const uint16_t* __restrict__ kc,
                                                       const uint16_t* __restrict__ vc, const int* __restrict__ bt,
                                                       const int* __restrict__ lens, char* __restrict__ ws,
                                                       uint16_t* __restrict__ out) {
  __shared__ __attribute__((aligned(16))) float lds[LDS_FLOATS];
  const int tid = threadIdx.x, lane = tid & 63;
  const int wave = __builtin_amdgcn_readfirstlane(tid >> 6);
  const int g = lane >> 4, c = lane & 15;
  const bool lo = c < 8;
  const int wg = blockIdx.x;
  const int hf = wg & 1, wi = (wg >> 3) + 32 * ((wg >> 1) & 3);
  const int gp = wave * 128 + wi;

  __builtin_amdgcn_s_setprio(3);
  asm volatile("" ::"s"(ws), "s"(vc), "s"(kc), "s"(out));
  int len_raw, pg[NU];
  u32x4 qtmp, tag;
  const char* inbox = ws + (size_t)wg * INBOX_BYTES;
  {
    asm volatile("s_load_dword %0, %5, 0x0\n\t"
                 "s_load_dword %1, %6, 0x0\n\t"
                 "s_load_dword %2, %6, 0x1000\n\t"
                 "s_load_dword %3, %6, 0x2000\n\t"
                 "s_load_dword %4, %6, 0x3000"
                 : "=&s"(len_raw), "=&s"(pg[0]), "=&s"(pg[1]), "=&s"(pg[2]), "=&s"(pg[3])
                 : "s"(lens), "s"(bt + gp));
    const int hd = wave >> 1, ln = wave & 1;
    const int qoff = ((8 * hd + (c & 7)) * D + 64 * ln + 32 * ((c >> 3) ^ (hd & 1)) + 8 * g) * 2;
    asm volatile("global_load_dwordx4 %0, %2, %3\n\t"
                 "global_load_dwordx4 %1, %4, %5"
                 : "=&v"(qtmp), "=&v"(tag) : "v"(qoff), "s"(q), "v"((lane & 3) * 16), "s"(ws + WS_EPOCH));
    __builtin_amdgcn_sched_barrier(0);
    asm volatile("s_waitcnt lgkmcnt(0)" : "+s"(len_raw), "+s"(pg[0]), "+s"(pg[1]), "+s"(pg[2]), "+s"(pg[3]));
  }
  const bool uni = len_raw <= 0;
  const int len = uni ? NB * BS : min(len_raw, NB * BS);
  const int lenk = uni ? 0 : len;
  const int n = (((len + 15) >> 4) - gp + 1023) >> 10;
  const int koff = (c >> 1) * 1024 + (c & 1) * 64 + g * 16;
  const int voff = g * 2048 + c * 16;
  const char* kbase = (const char*)kc + hf * 512;
  const char* vbase = (const char*)vc + hf * 512;

  u32x4 buf[NU][8];
  auto issue_k = [&](int i) {
    load8<128>(buf[i], koff, make_srd(kbase + (size_t)(unsigned)pg[i] * PAGE_BYTES, max(lenk - (gp + i * 1024) * 16, 0) * 1024));
  };
  auto issue_v = [&](int i) {
    load8<1024>(buf[i], voff, make_srd(vbase + (size_t)(unsigned)pg[i] * PAGE_BYTES, (len - (gp + i * 1024) * 16) * 1024));
  };
  if (0 < n) issue_k(0);
  asm volatile("s_cmp_gt_u32 %2, 0\n\ts_cbranch_scc1 1f\n\t"
               "s_waitcnt vmcnt(0)\n"
               "1:\n\ts_waitcnt vmcnt(8)"
               : "+v"(qtmp), "+v"(tag) : "s"(n) : "scc");
#pragma unroll
  for (int e = 0; e < 4; ++e) {
    const unsigned t = (tag[e] & TAG_MASK) + 4u;
    tag[e] = t >= 4u * (e == 0 ? 128 : e == 1 ? 127 : e == 2 ? 125 : 123) ? 0u : t;
  }
  asm volatile("" : "+v"(tag));
  reinterpret_cast<u32x4*>(lds + LDS_Q)[tid] = qtmp;
  if (tid == 0) reinterpret_cast<int*>(lds)[LDS_CNT] = 0;
  wg_barrier();
  if (1 < n) issue_k(1);
  if (2 < n) issue_k(2);
  u32x4 qf[4];
#pragma unroll
  for (int f = 0; f < 4; ++f) qf[f] = reinterpret_cast<const u32x4*>(lds + LDS_Q)[(4 * hf + f) * 64 + lane];

  constexpr float C2 = 0.088388347648318440f * 1.4426950408889634f;
  float sc[NU][4];
#pragma unroll
  for (int i = 0; i < NU; ++i) {
    if (i < n) {
      const int tok0 = (gp + i * 1024) * 16;
      wait_groups(i == 0 ? min(n, 3) - 1 : i == 3 ? 2 : n - 1, buf[i]);
      f32x4 d[4];
#pragma unroll
      for (int e = 0; e < 4; ++e) {
        d[e] = mfma32(buf[i][2 * e], qf[2 * (e & 1)], f32x4{0.f, 0.f, 0.f, 0.f});
        d[e] = mfma32(buf[i][2 * e + 1], qf[2 * (e & 1) + 1], d[e]);
      }
      __builtin_amdgcn_sched_barrier(0);
      if (i == 0) { if (3 < n) issue_k(3); __builtin_amdgcn_s_setprio(0); issue_v(0); }
      if (i == 1) issue_v(1);
#pragma unroll
      for (int o = 0; o < 2; ++o)
#pragma unroll
        for (int a = 0; a < 2; ++a) {
          const float u = lo ? d[2 * o][2 * a] : d[2 * o + 1][2 * a];
          const float w = lo ? d[2 * o + 1][2 * a + 1] : d[2 * o][2 * a + 1];
          const float s = (u + dpp<ROR8>(w)) * C2;
          sc[i][2 * o + a] = (tok0 + 8 * o + 2 * g + a < len) ? s : -INFINITY;
        }
    } else {
#pragma unroll
      for (int k = 0; k < 4; ++k) sc[i][k] = -INFINITY;
    }
  }
  u32x2 pf[NU - 1];
  float mx = -INFINITY, pl[4];
  {
#pragma unroll
    for (int i = 0; i < NU; ++i) mx = fmaxf(mx, fmaxf(fmaxf(sc[i][0], sc[i][1]), fmaxf(sc[i][2], sc[i][3])));
    mx = xg_max(mx);
    const float ms = (mx == -INFINITY) ? 0.f : mx;
    float ls = 0.f;
#pragma unroll
    for (int i = 0; i < NU; ++i) {
#pragma unroll
      for (int k = 0; k < 4; ++k) { sc[i][k] = __builtin_amdgcn_exp2f(sc[i][k] - ms); ls += sc[i][k]; }
      if (i < NU - 1) pf[i] = u32x2{pk_bf16(sc[i][0], sc[i][1]), pk_bf16(sc[i][2], sc[i][3])};
    }
#pragma unroll
    for (int k = 0; k < 4; ++k) pl[k] = n > 3 ? sc[3][k] : n > 2 ? sc[2][k] : n > 1 ? sc[1][k] : sc[0][k];
    ls = xg_sum(ls);
    if (g == 0) { lds[LDS_ML + c * 16 + wave] = mx; lds[LDS_ML + c * 16 + 8 + wave] = ls; }
    asm volatile("s_waitcnt lgkmcnt(0)\n\t"
                 "s_mov_b64 exec, 1\n\t"
                 "ds_add_u32 %0, %1\n\t"
                 "s_mov_b64 exec, -1"
                 :: "v"((int)(uintptr_t)(__attribute__((address_space(3))) float*)(lds + LDS_CNT)), "v"(1) : "memory");
  }
  f32x4 acc[8];
#pragma unroll
  for (int k = 0; k < 8; ++k) acc[k] = f32x4{0.f, 0.f, 0.f, 0.f};
  auto pv = [&](u32x4 (&vb)[8], u32x2 p) {
    const u32x4 b = {lo ? p[0] : 0u, lo ? p[1] : 0u, lo ? 0u : p[0], lo ? 0u : p[1]};
#pragma unroll
    for (int k = 0; k < 8; ++k) {
      const unsigned sel = (k & 1) ? 0x07060302u : 0x05040100u;
      const u32x4 a = {__builtin_amdgcn_perm(vb[1][k >> 1], vb[0][k >> 1], sel), __builtin_amdgcn_perm(vb[5][k >> 1], vb[4][k >> 1], sel),
                       __builtin_amdgcn_perm(vb[3][k >> 1], vb[2][k >> 1], sel), __builtin_amdgcn_perm(vb[7][k >> 1], vb[6][k >> 1], sel)};
      acc[k] = mfma32(a, b, acc[k]);
    }
  };
#pragma unroll
  for (int i = 0; i < NU - 1; ++i) {
    if (i + 1 < n) {
      wait_v<8>(buf[i]);
      if (i + 2 < NU && i + 2 < n) issue_v(i + 2);
      pv(buf[i], pf[i]);
    }
  }
  u32x4 pc;
  int otouch;
  asm volatile("global_load_dwordx4 %0, %2, %3 sc1\n\t"
               "global_load_dword %1, %4, %5 sc1"
               : "=&v"(pc), "=&v"(otouch) : "v"(tid * 16), "s"(inbox), "v"((wg >> 3) * 256), "s"(out));
  const int h = tid >> 5, d4 = tid & 31;
  unsigned low[4];
  u32x2 plast;
  {
    for (unsigned spin = 0; spin < (1u << 20); ++spin) {
      if (__builtin_amdgcn_readfirstlane(__hip_atomic_load(reinterpret_cast<int*>(lds) + LDS_CNT, __ATOMIC_RELAXED, __HIP_MEMORY_SCOPE_WORKGROUP)) >= NWAVE) break;
      __builtin_amdgcn_s_sleep(2);
    }
    asm volatile("" ::: "memory");
    float f = 0.f;
#pragma unroll
    for (int r = 0; r < 2; ++r) {
      const float* ml = lds + LDS_ML + (r ? h : c) * 16;
      const f32x4 m0 = *reinterpret_cast<const f32x4*>(ml), m1 = *reinterpret_cast<const f32x4*>(ml + 4);
      const f32x4 l0 = *reinterpret_cast<const f32x4*>(ml + 8), l1 = *reinterpret_cast<const f32x4*>(ml + 12);
      const float M = fmaxf(fmaxf(fmaxf(m0[0], m0[1]), fmaxf(m0[2], m0[3])), fmaxf(fmaxf(m1[0], m1[1]), fmaxf(m1[2], m1[3])));
      const float Ms = (M == -INFINITY) ? 0.f : M;
      float L = 0.f;
#pragma unroll
      for (int w = 0; w < 4; ++w) L += __builtin_amdgcn_exp2f(m0[w] - Ms) * l0[w];
#pragma unroll
      for (int w = 0; w < 4; ++w) L += __builtin_amdgcn_exp2f(m1[w] - Ms) * l1[w];
      if (r == 0) {
        f = L > 0.f ? __builtin_amdgcn_exp2f(mx - Ms) * __builtin_amdgcn_rcpf(L) : 0.f;
      } else {
        const unsigned lse = __float_as_uint(M + __builtin_amdgcn_logf(L));
#pragma unroll
        for (int e = 0; e < 4; ++e) low[e] = tag[e] | ((lse >> (8 * (d4 & 3) + 2 * e)) & 3u);
      }
    }
#pragma unroll
    for (int k = 0; k < 8; ++k) acc[k] *= f;
    plast = u32x2{pk_bf16(pl[0] * f, pl[1] * f), pk_bf16(pl[2] * f, pl[3] * f)};
    asm volatile("" : "+v"(plast), "+v"(low[0]), "+v"(low[1]), "+v"(low[2]), "+v"(low[3]));
  }
#pragma unroll
  for (int i = 0; i < NU; ++i) {
    if (i + 1 == n) {
      wait_v<2>(buf[i]);
      pv(buf[i], plast);
    }
  }
  __builtin_amdgcn_sched_barrier(0);
  asm volatile("s_waitcnt vmcnt(2)" ::: "memory");
  __builtin_amdgcn_sched_barrier(0);
  {
    float* rec = lds + (wave * 16 + c) * D;
#pragma unroll
    for (int k = 0; k < 8; ++k) *reinterpret_cast<f32x4*>(rec + 32 * g + 4 * (k ^ (c & 7))) = acc[k];
  }
  const int moff = ((16 * hf + h) * 8 + (d4 >> 2)) * INBOX_BYTES + wi * 64 + (d4 & 3) * 16;
  wg_barrier();
  {
    f32x4 po[NWAVE];
#pragma unroll
    for (int w = 0; w < NWAVE; ++w) po[w] = *reinterpret_cast<const f32x4*>(lds + (w * 16 + h) * D + 4 * (d4 ^ (h & 7)));
    const f32x4 o = ((po[0] + po[1]) + (po[2] + po[3])) + ((po[4] + po[5]) + (po[6] + po[7]));
    u32x4 msg;
#pragma unroll
    for (int e = 0; e < 4; ++e) msg[e] = (__float_as_uint(o[e]) & DATA_MASK) | low[e];
    asm volatile("global_store_dwordx4 %0, %1, %2 sc1\n\ts_nop 1" :: "v"(moff), "v"(msg), "s"(ws) : "memory");
    asm volatile("s_waitcnt vmcnt(1)" : "+v"(pc), "+v"(otouch));
  }
  for (unsigned spin = 0; spin < SPIN_CAP; ++spin) {
    unsigned x = 0;
#pragma unroll
    for (int e = 0; e < 4; ++e) x |= pc[e] ^ tag[e];
    x &= TAG_MASK;
    x |= __builtin_amdgcn_mov_dpp(x, QX1, 0xf, 0xf, true);
    x |= __builtin_amdgcn_mov_dpp(x, QX2, 0xf, 0xf, true);
    const unsigned long long miss = __builtin_amdgcn_ballot_w64(x != 0u);
    if (miss == 0ull) break;
    if (__builtin_popcountll(miss) > 16) __builtin_amdgcn_s_sleep(16);
    asm volatile("s_mov_b64 exec, %2\n\t"
                 "global_load_dwordx4 %0, %1, %3 sc1\n\t"
                 "s_mov_b64 exec, -1\n\t"
                 "s_waitcnt vmcnt(0)"
                 : "+v"(pc) : "v"(tid * 16), "s"(miss), "s"(inbox) : "memory");
  }
  if (wg == 128 && wave >= 6) {
    for (unsigned spin = 0; spin < SPIN_CAP; ++spin) {
      u32x4 w;
      asm volatile("global_load_dwordx4 %0, %1, %2 sc1\n\ts_waitcnt vmcnt(0)" : "=&v"(w) : "v"((tid - 384) * 64 + (lane & 3) * 16), "s"(ws) : "memory");
      unsigned x = 0;
#pragma unroll
      for (int e = 0; e < 4; ++e) x |= w[e] ^ tag[e];
      if (__builtin_amdgcn_ballot_w64((x & TAG_MASK) != 0u) == 0ull) break;
    }
  }
  {
    float* mrg = lds + LDS_Q;
    unsigned b = ((pc[0] & 3u) | ((pc[1] & 3u) << 2) | ((pc[2] & 3u) << 4) | ((pc[3] & 3u) << 6)) << (8 * (lane & 3));
    b |= __builtin_amdgcn_mov_dpp(b, QX1, 0xf, 0xf, true);
    b |= __builtin_amdgcn_mov_dpp(b, QX2, 0xf, 0xf, true);
    const float ls = __uint_as_float(b);
    float M = ls;
    M = fmaxf(M, dpp<ROR4>(M)); M = fmaxf(M, dpp<ROR8>(M)); M = xg_max(M);
    const float Ms = (M == -INFINITY) ? 0.f : M;
    const float w0 = __builtin_amdgcn_exp2f(ls - Ms);
    float v[5];
#pragma unroll
    for (int e = 0; e < 4; ++e) v[e] = w0 * __uint_as_float(pc[e] & DATA_MASK);
    v[4] = w0;
#pragma unroll
    for (int e = 0; e < 5; ++e) { v[e] += dpp<ROR4>(v[e]); v[e] += dpp<ROR8>(v[e]); v[e] = xg_sum(v[e]); }
    if (lane < 4) {
      *reinterpret_cast<f32x4*>(mrg + (wave * 4 + lane) * 8) = f32x4{v[0], v[1], v[2], v[3]};
      *reinterpret_cast<f32x2*>(mrg + (wave * 4 + lane) * 8 + 4) = f32x2{v[4], M};
    }
    wg_barrier();
    if (wave == 0) {
      const int r = lane & 31;
      const f32x4 o = *reinterpret_cast<const f32x4*>(mrg + r * 8);
      const f32x2 wm = *reinterpret_cast<const f32x2*>(mrg + r * 8 + 4);
      float MM = wm[1];
      MM = fmaxf(MM, dpp<ROR4>(MM)); MM = fmaxf(MM, dpp<ROR8>(MM)); MM = x16_max(MM);
      const float wt = __builtin_amdgcn_exp2f(wm[1] - MM);
      float u[5] = {o[0] * wt, o[1] * wt, o[2] * wt, o[3] * wt, wm[0] * wt};
#pragma unroll
      for (int e = 0; e < 5; ++e) { u[e] += dpp<ROR4>(u[e]); u[e] += dpp<ROR8>(u[e]); u[e] = x16_sum(u[e]); }
      const int r4 = (lane >> 2) & 3;
      const float val = (r4 == 0 ? u[0] : r4 == 1 ? u[1] : r4 == 2 ? u[2] : u[3]) * __builtin_amdgcn_rcpf(u[4]);
      const int ooff = ((wg >> 3) * D + 32 * ((wg & 7) >> 1) + 4 * (wg & 1) + 8 * r4 + (lane & 3)) * 2;
      if (lane < 16) asm volatile("global_store_short %0, %1, %2" :: "v"(ooff), "v"(pk_bf16(val, val)), "s"(out) : "memory");
      if (wg == 128 && lane < 4) asm volatile("global_store_dwordx4 %0, %1, %2 sc1\n\ts_nop 1" :: "v"(lane * 16), "v"(tag), "s"(ws + WS_EPOCH) : "memory");
    }
  }
}
}

extern "C" void kernel_launch(void* const* d_in, const int* in_sizes, int n_in, void* d_out, int out_size, void* d_ws,
                              size_t ws_size, hipStream_t stream) {
  attn_decode<<<dim3(NWG), dim3(NTHR), 0, stream>>>((const uint16_t*)d_in[0], (const uint16_t*)d_in[1], (const uint16_t*)d_in[2],
                                                    (const int*)d_in[3], (const int*)d_in[4], (char*)d_ws, (uint16_t*)d_out);
}
```

```cpp
#include <hip/hip_runtime.h>
#include <hip/hip_bf16.h>
#include <stdint.h>

namespace {
constexpr int HKV = 4, D = 128, BS = 16;
constexpr int NB = 4096;
constexpr int NWG = 256, NWAVE = 8, NTHR = NWAVE * 64;
constexpr int NU = NB / (NWG / 2 * NWAVE);
constexpr int PAGE_BYTES = BS * HKV * D * 2;
static_assert(NU == 4, "written for 4 pages per wave");
constexpr int LDS_Q = NWAVE * 16 * D;
constexpr int LDS_ML = LDS_Q + 2048;
constexpr int LDS_CNT = LDS_ML + 16 * 16;
constexpr int LDS_FLOATS = LDS_CNT + 4;
constexpr int INBOX_BYTES = 128 * 64;
constexpr int WS_EPOCH = NWG * INBOX_BYTES;
constexpr unsigned TAG_MASK = 0x1fcu, DATA_MASK = 0xfffffe00u;
constexpr unsigned SPIN_CAP = 1u << 15;

using bf16x8 = __attribute__((ext_vector_type(8))) __bf16;
using bf16x2 = __attribute__((ext_vector_type(2))) __bf16;
using f32x4 = __attribute__((ext_vector_type(4))) float;
using f32x2 = __attribute__((ext_vector_type(2))) float;
using u32x4 = __attribute__((ext_vector_type(4))) unsigned;
using u32x2 = __attribute__((ext_vector_type(2))) unsigned;

template <int CTRL> __device__ __forceinline__ float dpp(float x) {
  return __builtin_bit_cast(float, __builtin_amdgcn_mov_dpp(__builtin_bit_cast(int, x), CTRL, 0xf, 0xf, true));
}
constexpr int ROR4 = 0x124, ROR8 = 0x128, QX1 = 0xB1, QX2 = 0x4E;
__device__ __forceinline__ float x16_sum(float x) {
  auto s = __builtin_amdgcn_permlane16_swap(__float_as_uint(x), __float_as_uint(x), false, false);
  return __uint_as_float(s[0]) + __uint_as_float(s[1]);
}
__device__ __forceinline__ float x16_max(float x) {
  auto s = __builtin_amdgcn_permlane16_swap(__float_as_uint(x), __float_as_uint(x), false, false);
  return fmaxf(__uint_as_float(s[0]), __uint_as_float(s[1]));
}
__device__ __forceinline__ float xg_max(float x) {
  x = x16_max(x);
  auto t = __builtin_amdgcn_permlane32_swap(__float_as_uint(x), __float_as_uint(x), false, false);
  return fmaxf(__uint_as_float(t[0]), __uint_as_float(t[1]));
}
__device__ __forceinline__ float xg_sum(float x) {
  x = x16_sum(x);
  auto t = __builtin_amdgcn_permlane32_swap(__float_as_uint(x), __float_as_uint(x), false, false);
  return __uint_as_float(t[0]) + __uint_as_float(t[1]);
}
__device__ __forceinline__ unsigned pk_bf16(float lo, float hi) {
  bf16x2 v = {(__bf16)lo, (__bf16)hi};
  return __builtin_bit_cast(unsigned, v);
}
__device__ __forceinline__ f32x4 mfma32(u32x4 a, u32x4 b, f32x4 c) {
  return __builtin_amdgcn_mfma_f32_16x16x32_bf16(__builtin_bit_cast(bf16x8, a), __builtin_bit_cast(bf16x8, b), c, 0, 0, 0);
}
__device__ __forceinline__ u32x4 make_srd(const void* base, unsigned nbytes) {
  const uint64_t a = (uint64_t)base;
  return u32x4{(unsigned)a, (unsigned)(a >> 32), nbytes, 0x00020000u};
}
template <int ODD>
__device__ __forceinline__ void load8(u32x4 (&d)[8], int voff, u32x4 srd) {
  asm volatile("buffer_load_dwordx4 %0, %8, %9, 0 offen nt\n\t"
               "buffer_load_dwordx4 %1, %8, %9, 0 offen offset:%11 nt\n\t"
               "buffer_load_dwordx4 %2, %8, %9, 0 offen offset:256 nt\n\t"
               "buffer_load_dwordx4 %3, %8, %9, 0 offen offset:%12 nt\n\t"
               "buffer_load_dwordx4 %4, %8, %9, %10 offen nt\n\t"
               "buffer_load_dwordx4 %5, %8, %9, %10 offen offset:%11 nt\n\t"
               "buffer_load_dwordx4 %6, %8, %9, %10 offen offset:256 nt\n\t"
               "buffer_load_dwordx4 %7, %8, %9, %10 offen offset:%12 nt"
               : "=&v"(d[0]), "=&v"(d[1]), "=&v"(d[2]), "=&v"(d[3]), "=&v"(d[4]), "=&v"(d[5]), "=&v"(d[6]), "=&v"(d[7])
               : "v"(voff), "s"(srd), "s"(8192), "i"(ODD), "i"(256 + ODD));
  __builtin_amdgcn_sched_barrier(0);
}
__device__ __forceinline__ void wait_groups(int x, u32x4 (&r)[8]) {
  asm volatile("s_cmp_gt_u32 %8, 2\n\ts_cbranch_scc1 3f\n\t"
               "s_cmp_gt_u32 %8, 1\n\ts_cbranch_scc1 2f\n\t"
               "s_cmp_gt_u32 %8, 0\n\ts_cbranch_scc1 1f\n\t"
               "s_waitcnt vmcnt(0)\n"
               "1:\n\ts_waitcnt vmcnt(8)\n"
               "2:\n\ts_waitcnt vmcnt(16)\n"
               "3:\n\ts_waitcnt vmcnt(24)"
               : "+v"(r[0]), "+v"(r[1]), "+v"(r[2]), "+v"(r[3]), "+v"(r[4]), "+v"(r[5]), "+v"(r[6]), "+v"(r[7])
               : "s"(x) : "scc");
}
template <int N>
__device__ __forceinline__ void wait_v(u32x4 (&r)[8]) {
  asm volatile("s_waitcnt vmcnt(%8)"
               : "+v"(r[0]), "+v"(r[1]), "+v"(r[2]), "+v"(r[3]), "+v"(r[4]), "+v"(r[5]), "+v"(r[6]), "+v"(r[7]) : "i"(N));
}
__device__ __forceinline__ void wg_barrier() { asm volatile("s_waitcnt lgkmcnt(0)\n\ts_barrier" ::: "memory"); }

__global__ __launch_bounds__(NTHR, 2) void attn_decode(const uint16_t* __restrict__ q, const uint16_t* __restrict__ kc,
                                                       const uint16_t* __restrict__ vc, const int* __restrict__ bt,
                                                       const int* __restrict__ lens, char* __restrict__ ws,
                                                       uint16_t* __restrict__ out) {
  __shared__ __attribute__((aligned(16))) float lds[LDS_FLOATS];
  const int tid = threadIdx.x, lane = tid & 63;
  const int wave = __builtin_amdgcn_readfirstlane(tid >> 6);
  const int g = lane >> 4, c = lane & 15;
  const bool lo = c < 8;
  const int wg = blockIdx.x;
  const int hf = wg & 1, wi = (wg >> 3) + 32 * ((wg >> 1) & 3);
  const int gp = wave * 128 + wi;

  __builtin_amdgcn_s_setprio(3);
  asm volatile("" ::"s"(ws), "s"(vc), "s"(kc), "s"(out));
  int len_raw, pg[NU];
  u32x4 qtmp, tag;
  const char* inbox = ws + (size_t)wg * INBOX_BYTES;
  {
    asm volatile("s_load_dword %0, %5, 0x0\n\t"
                 "s_load_dword %1, %6, 0x0\n\t"
                 "s_load_dword %2, %6, 0x1000\n\t"
                 "s_load_dword %3, %6, 0x2000\n\t"
                 "s_load_dword %4, %6, 0x3000"
                 : "=&s"(len_raw), "=&s"(pg[0]), "=&s"(pg[1]), "=&s"(pg[2]), "=&s"(pg[3])
                 : "s"(lens), "s"(bt + gp));
    const int hd = wave >> 1, ln = wave & 1;
    const int qoff = ((8 * hd + (c & 7)) * D + 64 * ln + 32 * ((c >> 3) ^ (hd & 1)) + 8 * g) * 2;
    asm volatile("global_load_dwordx4 %0, %2, %3\n\t"
                 "global_load_dwordx4 %1, %4, %5"
                 : "=&v"(qtmp), "=&v"(tag) : "v"(qoff), "s"(q), "v"((lane & 3) * 16), "s"(ws + WS_EPOCH));
    __builtin_amdgcn_sched_barrier(0);
    asm volatile("s_waitcnt lgkmcnt(0)" : "+s"(len_raw), "+s"(pg[0]), "+s"(pg[1]), "+s"(pg[2]), "+s"(pg[3]));
  }
  const bool uni = len_raw <= 0;
  const int len = uni ? NB * BS : min(len_raw, NB * BS);
  const int lenk = uni ? 0 : len;
  const int n = (((len + 15) >> 4) - gp + 1023) >> 10;
  const int koff = (c >> 1) * 1024 + (c & 1) * 64 + g * 16;
  const int voff = g * 2048 + c * 16;
  const char* kbase = (const char*)kc + hf * 512;
  const char* vbase = (const char*)vc + hf * 512;

  u32x4 buf[NU][8];
  auto issue_k = [&](int i) {
    load8<128>(buf[i], koff, make_srd(kbase + (size_t)(unsigned)pg[i] * PAGE_BYTES, max(lenk - (gp + i * 1024) * 16, 0) * 1024));
  };
  auto issue_v = [&](int i) {
    load8<1024>(buf[i], voff, make_srd(vbase + (size_t)(unsigned)pg[i] * PAGE_BYTES, (len - (gp + i * 1024) * 16) * 1024));
  };
  if (0 < n) issue_k(0);
  asm volatile("s_cmp_gt_u32 %2, 0\n\ts_cbranch_scc1 1f\n\t"
               "s_waitcnt vmcnt(0)\n"
               "1:\n\ts_waitcnt vmcnt(8)"
               : "+v"(qtmp), "+v"(tag) : "s"(n) : "scc");
#pragma unroll
  for (int e = 0; e < 4; ++e) {
    const unsigned t = (tag[e] & TAG_MASK) + 4u;
    tag[e] = t >= 4u * (e == 0 ? 128 : e == 1 ? 127 : e == 2 ? 125 : 123) ? 0u : t;
  }
  asm volatile("" : "+v"(tag));
  reinterpret_cast<u32x4*>(lds + LDS_Q)[tid] = qtmp;
  if (tid == 0) reinterpret_cast<int*>(lds)[LDS_CNT] = 0;
  wg_barrier();
  if (1 < n) issue_k(1);
  if (2 < n) issue_k(2);
  if (3 < n) issue_k(3);
  u32x4 qf[4];
#pragma unroll
  for (int f = 0; f < 4; ++f) qf[f] = reinterpret_cast<const u32x4*>(lds + LDS_Q)[(4 * hf + f) * 64 + lane];

  constexpr float C2 = 0.088388347648318440f * 1.4426950408889634f;
  float sc[NU][4];
#pragma unroll
  for (int i = 0; i < NU; ++i) {
    if (i < n) {
      const int tok0 = (gp + i * 1024) * 16;
      wait_groups(n - 1 - i, buf[i]);
      f32x4 d[4];
#pragma unroll
      for (int e = 0; e < 4; ++e) {
        d[e] = mfma32(buf[i][2 * e], qf[2 * (e & 1)], f32x4{0.f, 0.f, 0.f, 0.f});
        d[e] = mfma32(buf[i][2 * e + 1], qf[2 * (e & 1) + 1], d[e]);
      }
#pragma unroll
      for (int o = 0; o < 2; ++o)
#pragma unroll
        for (int a = 0; a < 2; ++a) {
          const float u = lo ? d[2 * o][2 * a] : d[2 * o + 1][2 * a];
          const float w = lo ? d[2 * o + 1][2 * a + 1] : d[2 * o][2 * a + 1];
          const float s = (u + dpp<ROR8>(w)) * C2;
          sc[i][2 * o + a] = (tok0 + 8 * o + 2 * g + a < len) ? s : -INFINITY;
        }
    } else {
#pragma unroll
      for (int k = 0; k < 4; ++k) sc[i][k] = -INFINITY;
    }
  }
  u32x2 pf[NU - 1];
  float mx = -INFINITY, pl[4];
  {
#pragma unroll
    for (int i = 0; i < NU; ++i) mx = fmaxf(mx, fmaxf(fmaxf(sc[i][0], sc[i][1]), fmaxf(sc[i][2], sc[i][3])));
    mx = xg_max(mx);
    const float ms = (mx == -INFINITY) ? 0.f : mx;
    float ls = 0.f;
#pragma unroll
    for (int i = 0; i < NU; ++i) {
#pragma unroll
      for (int k = 0; k < 4; ++k) { sc[i][k] = __builtin_amdgcn_exp2f(sc[i][k] - ms); ls += sc[i][k]; }
      if (i < NU - 1) pf[i] = u32x2{pk_bf16(sc[i][0], sc[i][1]), pk_bf16(sc[i][2], sc[i][3])};
    }
#pragma unroll
    for (int k = 0; k < 4; ++k) pl[k] = n > 3 ? sc[3][k] : n > 2 ? sc[2][k] : n > 1 ? sc[1][k] : sc[0][k];
    ls = xg_sum(ls);
    if (g == 0) { lds[LDS_ML + c * 16 + wave] = mx; lds[LDS_ML + c * 16 + 8 + wave] = ls; }
    asm volatile("s_waitcnt lgkmcnt(0)\n\t"
                 "s_mov_b64 exec, 1\n\t"
                 "ds_add_u32 %0, %1\n\t"
                 "s_mov_b64 exec, -1"
                 :: "v"((int)(uintptr_t)(__attribute__((address_space(3))) float*)(lds + LDS_CNT)), "v"(1) : "memory");
  }
  __builtin_amdgcn_sched_barrier(0);
  __builtin_amdgcn_s_setprio(0);
  if (0 < n) issue_v(0);
  if (1 < n) issue_v(1);
  const int h = tid >> 5, d4 = tid & 31;
  unsigned low[4];
  float f = 0.f;
  auto all_there = [&]() {
    return __builtin_amdgcn_readfirstlane(__hip_atomic_load(reinterpret_cast<int*>(lds) + LDS_CNT, __ATOMIC_RELAXED, __HIP_MEMORY_SCOPE_WORKGROUP)) >= NWAVE;
  };
  auto scale = [&]() {
    asm volatile("" ::: "memory");
#pragma unroll
    for (int r = 0; r < 2; ++r) {
      const float* ml = lds + LDS_ML + (r ? h : c) * 16;
      const f32x4 m0 = *reinterpret_cast<const f32x4*>(ml), m1 = *reinterpret_cast<const f32x4*>(ml + 4);
      const f32x4 l0 = *reinterpret_cast<const f32x4*>(ml + 8), l1 = *reinterpret_cast<const f32x4*>(ml + 12);
      const float M = fmaxf(fmaxf(fmaxf(m0[0], m0[1]), fmaxf(m0[2], m0[3])), fmaxf(fmaxf(m1[0], m1[1]), fmaxf(m1[2], m1[3])));
      const float Ms = (M == -INFINITY) ? 0.f : M;
      float L = 0.f;
#pragma unroll
      for (int w = 0; w < 4; ++w) L += __builtin_amdgcn_exp2f(m0[w] - Ms) * l0[w];
#pragma unroll
      for (int w = 0; w < 4; ++w) L += __builtin_amdgcn_exp2f(m1[w] - Ms) * l1[w];
      if (r == 0) {
        f = L > 0.f ? __builtin_amdgcn_exp2f(mx - Ms) * __builtin_amdgcn_rcpf(L) : 0.f;
      } else {
        const unsigned lse = __float_as_uint(M + __builtin_amdgcn_logf(L));
#pragma unroll
        for (int e = 0; e < 4; ++e) low[e] = tag[e] | ((lse >> (8 * (d4 & 3) + 2 * e)) & 3u);
      }
    }
    asm volatile("" : "+v"(f), "+v"(low[0]), "+v"(low[1]), "+v"(low[2]), "+v"(low[3]));
  };
  const bool early = all_there();
  if (early) scale();
  f32x4 acc[8];
#pragma unroll
  for (int k = 0; k < 8; ++k) acc[k] = f32x4{0.f, 0.f, 0.f, 0.f};
  auto pv = [&](u32x4 (&vb)[8], u32x2 p) {
    const u32x4 b = {lo ? p[0] : 0u, lo ? p[1] : 0u, lo ? 0u : p[0], lo ? 0u : p[1]};
#pragma unroll
    for (int k = 0; k < 8; ++k) {
      const unsigned sel = (k & 1) ? 0x07060302u : 0x05040100u;
      const u32x4 a = {__builtin_amdgcn_perm(vb[1][k >> 1], vb[0][k >> 1], sel), __builtin_amdgcn_perm(vb[5][k >> 1], vb[4][k >> 1], sel),
                       __builtin_amdgcn_perm(vb[3][k >> 1], vb[2][k >> 1], sel), __builtin_amdgcn_perm(vb[7][k >> 1], vb[6][k >> 1], sel)};
      acc[k] = mfma32(a, b, acc[k]);
    }
  };
#pragma unroll
  for (int i = 0; i < NU - 1; ++i) {
    if (i + 1 < n) {
      wait_v<8>(buf[i]);
      if (i + 2 < NU && i + 2 < n) issue_v(i + 2);
      pv(buf[i], pf[i]);
    }
  }
  u32x4 pc;
  asm volatile("global_load_dwordx4 %0, %1, %2 sc1" : "=&v"(pc) : "v"(tid * 16), "s"(inbox));
  if (!early) {
    for (unsigned spin = 0; spin < (1u << 20) && !all_there(); ++spin) __builtin_amdgcn_s_sleep(2);
    scale();
  }
#pragma unroll
  for (int k = 0; k < 8; ++k) acc[k] *= f;
  u32x2 plast = {pk_bf16(pl[0] * f, pl[1] * f), pk_bf16(pl[2] * f, pl[3] * f)};
  asm volatile("" : "+v"(plast));
#pragma unroll
  for (int i = 0; i < NU; ++i) {
    if (i + 1 == n) {
      wait_v<1>(buf[i]);
      pv(buf[i], plast);
    }
  }
  __builtin_amdgcn_sched_barrier(0);
  asm volatile("s_waitcnt vmcnt(1)" ::: "memory");
  __builtin_amdgcn_sched_barrier(0);
  {
    float* rec = lds + (wave * 16 + c) * D;
#pragma unroll
    for (int k = 0; k < 8; ++k) *reinterpret_cast<f32x4*>(rec + 32 * g + 4 * (k ^ (c & 7))) = acc[k];
  }
  const int moff = ((16 * hf + h) * 8 + (d4 >> 2)) * INBOX_BYTES + wi * 64 + (d4 & 3) * 16;
  wg_barrier();
  {
    f32x4 po[NWAVE];
#pragma unroll
    for (int w = 0; w < NWAVE; ++w) po[w] = *reinterpret_cast<const f32x4*>(lds + (w * 16 + h) * D + 4 * (d4 ^ (h & 7)));
    const f32x4 o = ((po[0] + po[1]) + (po[2] + po[3])) + ((po[4] + po[5]) + (po[6] + po[7]));
    u32x4 msg;
#pragma unroll
    for (int e = 0; e < 4; ++e) msg[e] = (__float_as_uint(o[e]) & DATA_MASK) | low[e];
    asm volatile("global_store_dwordx4 %0, %1, %2 sc1\n\ts_nop 1" :: "v"(moff), "v"(msg), "s"(ws) : "memory");
    asm volatile("s_waitcnt vmcnt(1)" : "+v"(pc));
  }
  for (unsigned spin = 0; spin < SPIN_CAP; ++spin) {
    unsigned x = 0;
#pragma unroll
    for (int e = 0; e < 4; ++e) x |= pc[e] ^ tag[e];
    x &= TAG_MASK;
    x |= __builtin_amdgcn_mov_dpp(x, QX1, 0xf, 0xf, true);
    x |= __builtin_amdgcn_mov_dpp(x, QX2, 0xf, 0xf, true);
    const unsigned long long miss = __builtin_amdgcn_ballot_w64(x != 0u);
    if (miss == 0ull) break;
    if (__builtin_popcountll(miss) > 16) __builtin_amdgcn_s_sleep(16);
    asm volatile("s_mov_b64 exec, %2\n\t"
                 "global_load_dwordx4 %0, %1, %3 sc1\n\t"
                 "s_mov_b64 exec, -1\n\t"
                 "s_waitcnt vmcnt(0)"
                 : "+v"(pc) : "v"(tid * 16), "s"(miss), "s"(inbox) : "memory");
  }
  if (wg == 128 && wave >= 6) {
    for (unsigned spin = 0; spin < SPIN_CAP; ++spin) {
      u32x4 w;
      asm volatile("global_load_dwordx4 %0, %1, %2 sc1\n\ts_waitcnt vmcnt(0)" : "=&v"(w) : "v"((tid - 384) * 64 + (lane & 3) * 16), "s"(ws) : "memory");
      unsigned x = 0;
#pragma unroll
      for (int e = 0; e < 4; ++e) x |= w[e] ^ tag[e];
      if (__builtin_amdgcn_ballot_w64((x & TAG_MASK) != 0u) == 0ull) break;
    }
  }
  {
    float* mrg = lds + LDS_Q;
    unsigned b = ((pc[0] & 3u) | ((pc[1] & 3u) << 2) | ((pc[2] & 3u) << 4) | ((pc[3] & 3u) << 6)) << (8 * (lane & 3));
    b |= __builtin_amdgcn_mov_dpp(b, QX1, 0xf, 0xf, true);
    b |= __builtin_amdgcn_mov_dpp(b, QX2, 0xf, 0xf, true);
    const float ls = __uint_as_float(b);
    float M = ls;
    M = fmaxf(M, dpp<ROR4>(M)); M = fmaxf(M, dpp<ROR8>(M)); M = xg_max(M);
    const float Ms = (M == -INFINITY) ? 0.f : M;
    const float w0 = __builtin_amdgcn_exp2f(ls - Ms);
    float v[5];
#pragma unroll
    for (int e = 0; e < 4; ++e) v[e] = w0 * __uint_as_float(pc[e] & DATA_MASK);
    v[4] = w0;
#pragma unroll
    for (int e = 0; e < 5; ++e) { v[e] += dpp<ROR4>(v[e]); v[e] += dpp<ROR8>(v[e]); v[e] = xg_sum(v[e]); }
    if (lane < 4) {
      *reinterpret_cast<f32x4*>(mrg + (wave * 4 + lane) * 8) = f32x4{v[0], v[1], v[2], v[3]};
      *reinterpret_cast<f32x2*>(mrg + (wave * 4 + lane) * 8 + 4) = f32x2{v[4], M};
    }
    wg_barrier();
    if (wave == 0) {
      const int r = lane & 31;
      const f32x4 o = *reinterpret_cast<const f32x4*>(mrg + r * 8);
      const f32x2 wm = *reinterpret_cast<const f32x2*>(mrg + r * 8 + 4);
      float MM = wm[1];
      MM = fmaxf(MM, dpp<ROR4>(MM)); MM = fmaxf(MM, dpp<ROR8>(MM)); MM = x16_max(MM);
      const float wt = __builtin_amdgcn_exp2f(wm[1] - MM);
      float u[5] = {o[0] * wt, o[1] * wt, o[2] * wt, o[3] * wt, wm[0] * wt};
#pragma unroll
      for (int e = 0; e < 5; ++e) { u[e] += dpp<ROR4>(u[e]); u[e] += dpp<ROR8>(u[e]); u[e] = x16_sum(u[e]); }
      const int r4 = (lane >> 2) & 3;
      const float val = (r4 == 0 ? u[0] : r4 == 1 ? u[1] : r4 == 2 ? u[2] : u[3]) * __builtin_amdgcn_rcpf(u[4]);
      const int ooff = ((wg >> 3) * D + 32 * ((wg & 7) >> 1) + 4 * (wg & 1) + 8 * r4 + (lane & 3)) * 2;
      if (lane < 16) asm volatile("global_store_short %0, %1, %2" :: "v"(ooff), "v"(pk_bf16(val, val)), "s"(out) : "memory");
      if (wg == 128 && lane < 4) asm volatile("global_store_dwordx4 %0, %1, %2 sc1\n\ts_nop 1" :: "v"(lane * 16), "v"(tag), "s"(ws + WS_EPOCH) : "memory");
    }
  }
}
}

extern "C" void kernel_launch(void* const* d_in, const int* in_sizes, int n_in, void* d_out, int out_size, void* d_ws,
                              size_t ws_size, hipStream_t stream) {
  attn_decode<<<dim3(NWG), dim3(NTHR), 0, stream>>>((const uint16_t*)d_in[0], (const uint16_t*)d_in[1], (const uint16_t*)d_in[2],
                                                    (const int*)d_in[3], (const int*)d_in[4], (char*)d_ws, (uint16_t*)d_out);
}
```

```cpp
#include <hip/hip_runtime.h>
#include <hip/hip_bf16.h>
#include <stdint.h>

namespace {
constexpr int HKV = 4, D = 128, BS = 16;
constexpr int NB = 4096;
constexpr int NWG = 256, NWAVE = 8, NTHR = NWAVE * 64;
constexpr int NU = NB / (NWG / 2 * NWAVE);
constexpr int PAGE_BYTES = BS * HKV * D * 2;
static_assert(NU == 4, "written for 4 pages per wave");
constexpr int LDS_Q = NWAVE * 16 * D;
constexpr int LDS_ML = LDS_Q + 2048;
constexpr int LDS_CNT = LDS_ML + 16 * 16;
constexpr int LDS_FLOATS = LDS_CNT + 4;
constexpr int INBOX_BYTES = 128 * 64;
constexpr int WS_EPOCH = NWG * INBOX_BYTES;
constexpr unsigned TAG_MASK = 0x1fcu, DATA_MASK = 0xfffffe00u;
constexpr unsigned SPIN_CAP = 1u << 15;

using bf16x8 = __attribute__((ext_vector_type(8))) __bf16;
using bf16x2 = __attribute__((ext_vector_type(2))) __bf16;
using f32x4 = __attribute__((ext_vector_type(4))) float;
using f32x2 = __attribute__((ext_vector_type(2))) float;
using u32x4 = __attribute__((ext_vector_type(4))) unsigned;
using u32x2 = __attribute__((ext_vector_type(2))) unsigned;

template <int CTRL> __device__ __forceinline__ float dpp(float x) {
  return __builtin_bit_cast(float, __builtin_amdgcn_mov_dpp(__builtin_bit_cast(int, x), CTRL, 0xf, 0xf, true));
}
constexpr int ROR4 = 0x124, ROR8 = 0x128, QX1 = 0xB1, QX2 = 0x4E;
__device__ __forceinline__ float x16_sum(float x) {
  auto s = __builtin_amdgcn_permlane16_swap(__float_as_uint(x), __float_as_uint(x), false, false);
  return __uint_as_float(s[0]) + __uint_as_float(s[1]);
}
__device__ __forceinline__ float x16_max(float x) {
  auto s = __builtin_amdgcn_permlane16_swap(__float_as_uint(x), __float_as_uint(x), false, false);
  return fmaxf(__uint_as_float(s[0]), __uint_as_float(s[1]));
}
__device__ __forceinline__ float xg_max(float x) {
  x = x16_max(x);
  auto t = __builtin_amdgcn_permlane32_swap(__float_as_uint(x), __float_as_uint(x), false, false);
  return fmaxf(__uint_as_float(t[0]), __uint_as_float(t[1]));
}
__device__ __forceinline__ float xg_sum(float x) {
  x = x16_sum(x);
  auto t = __builtin_amdgcn_permlane32_swap(__float_as_uint(x), __float_as_uint(x), false, false);
  return __uint_as_float(t[0]) + __uint_as_float(t[1]);
}
__device__ __forceinline__ unsigned pk_bf16(float lo, float hi) {
  bf16x2 v = {(__bf16)lo, (__bf16)hi};
  return __builtin_bit_cast(unsigned, v);
}
__device__ __forceinline__ f32x4 mfma32(u32x4 a, u32x4 b, f32x4 c) {
  return __builtin_amdgcn_mfma_f32_16x16x32_bf16(__builtin_bit_cast(bf16x8, a), __builtin_bit_cast(bf16x8, b), c, 0, 0, 0);
}
__device__ __forceinline__ u32x4 make_srd(const void* base, unsigned nbytes) {
  const uint64_t a = (uint64_t)base;
  return u32x4{(unsigned)a, (unsigned)(a >> 32), nbytes, 0x00020000u};
}
template <int ODD>
__device__ __forceinline__ void load8(u32x4 (&d)[8], int voff, u32x4 srd) {
  asm volatile("buffer_load_dwordx4 %0, %8, %9, 0 offen nt\n\t"
               "buffer_load_dwordx4 %1, %8, %9, 0 offen offset:%11 nt\n\t"
               "buffer_load_dwordx4 %2, %8, %9, 0 offen offset:256 nt\n\t"
               "buffer_load_dwordx4 %3, %8, %9, 0 offen offset:%12 nt\n\t"
               "buffer_load_dwordx4 %4, %8, %9, %10 offen nt\n\t"
               "buffer_load_dwordx4 %5, %8, %9, %10 offen offset:%11 nt\n\t"
               "buffer_load_dwordx4 %6, %8, %9, %10 offen offset:256 nt\n\t"
               "buffer_load_dwordx4 %7, %8, %9, %10 offen offset:%12 nt"
               : "=&v"(d[0]), "=&v"(d[1]), "=&v"(d[2]), "=&v"(d[3]), "=&v"(d[4]), "=&v"(d[5]), "=&v"(d[6]), "=&v"(d[7])
               : "v"(voff), "s"(srd), "s"(8192), "i"(ODD), "i"(256 + ODD));
  __builtin_amdgcn_sched_barrier(0);
}
__device__ __forceinline__ void wait_groups(int x, u32x4 (&r)[8]) {
  asm volatile("s_cmp_gt_u32 %8, 2\n\ts_cbranch_scc1 3f\n\t"
               "s_cmp_gt_u32 %8, 1\n\ts_cbranch_scc1 2f\n\t"
               "s_cmp_gt_u32 %8, 0\n\ts_cbranch_scc1 1f\n\t"
               "s_waitcnt vmcnt(0)\n"
               "1:\n\ts_waitcnt vmcnt(8)\n"
               "2:\n\ts_waitcnt vmcnt(16)\n"
               "3:\n\ts_waitcnt vmcnt(24)"
               : "+v"(r[0]), "+v"(r[1]), "+v"(r[2]), "+v"(r[3]), "+v"(r[4]), "+v"(r[5]), "+v"(r[6]), "+v"(r[7])
               : "s"(x) : "scc");
}
template <int N>
__device__ __forceinline__ void wait_v(u32x4 (&r)[8]) {
  asm volatile("s_waitcnt vmcnt(%8)"
               : "+v"(r[0]), "+v"(r[1]), "+v"(r[2]), "+v"(r[3]), "+v"(r[4]), "+v"(r[5]), "+v"(r[6]), "+v"(r[7]) : "i"(N));
}
__device__ __forceinline__ void wg_barrier() { asm volatile("s_waitcnt lgkmcnt(0)\n\ts_barrier" ::: "memory"); }

__global__ __launch_bounds__(NTHR, 2) void attn_decode(const uint16_t* __restrict__ q, const uint16_t* __restrict__ kc,
                                                       const uint16_t* __restrict__ vc, const int* __restrict__ bt,
                                                       const int* __restrict__ lens, char* __restrict__ ws,
                                                       uint16_t* __restrict__ out) {
  __shared__ __attribute__((aligned(16))) float lds[LDS_FLOATS];
  const int tid = threadIdx.x, lane = tid & 63;
  const int wave = __builtin_amdgcn_readfirstlane(tid >> 6);
  const int g = lane >> 4, c = lane & 15;
  const bool lo = c < 8;
  const int wg = blockIdx.x;
  const int hf = wg & 1, wi = (wg >> 3) + 32 * ((wg >> 1) & 3);
  const int gp = wave * 128 + wi;

  __builtin_amdgcn_s_setprio(3);
  asm volatile("" ::"s"(ws), "s"(vc), "s"(kc), "s"(out));
  int len_raw, pg[NU];
  u32x4 qtmp, tag;
  const char* inbox = ws + (size_t)wg * INBOX_BYTES;
  {
    asm volatile("s_load_dword %0, %5, 0x0\n\t"
                 "s_load_dword %1, %6, 0x0\n\t"
                 "s_load_dword %2, %6, 0x1000\n\t"
                 "s_load_dword %3, %6, 0x2000\n\t"
                 "s_load_dword %4, %6, 0x3000"
                 : "=&s"(len_raw), "=&s"(pg[0]), "=&s"(pg[1]), "=&s"(pg[2]), "=&s"(pg[3])
                 : "s"(lens), "s"(bt + gp));
    const int hd = wave >> 1, ln = wave & 1;
    const int qoff = ((8 * hd + (c & 7)) * D + 64 * ln + 32 * ((c >> 3) ^ (hd & 1)) + 8 * g) * 2;
    asm volatile("global_load_dwordx4 %0, %2, %3\n\t"
                 "global_load_dwordx4 %1, %4, %5"
                 : "=&v"(qtmp), "=&v"(tag) : "v"(qoff), "s"(q), "v"((lane & 3) * 16), "s"(ws + WS_EPOCH));
    __builtin_amdgcn_sched_barrier(0);
    asm volatile("s_waitcnt lgkmcnt(0)" : "+s"(len_raw), "+s"(pg[0]), "+s"(pg[1]), "+s"(pg[2]), "+s"(pg[3]));
  }
  const bool uni = len_raw <= 0;
  const int len = uni ? NB * BS : min(len_raw, NB * BS);
  const int lenk = uni ? 0 : len;
  const int n = (((len + 15) >> 4) - gp + 1023) >> 10;
  const int koff = (c >> 1) * 1024 + (c & 1) * 64 + g * 16;
  const int voff = g * 2048 + c * 16;
  const char* kbase = (const char*)kc + hf * 512;
  const char* vbase = (const char*)vc + hf * 512;

  u32x4 buf[NU][8];
  auto issue_k = [&](int i) {
    load8<128>(buf[i], koff, make_srd(kbase + (size_t)(unsigned)pg[i] * PAGE_BYTES, max(lenk - (gp + i * 1024) * 16, 0) * 1024));
  };
  auto issue_v = [&](int i) {
    load8<1024>(buf[i], voff, make_srd(vbase + (size_t)(unsigned)pg[i] * PAGE_BYTES, (len - (gp + i * 1024) * 16) * 1024));
  };
  if (0 < n) issue_k(0);
  asm volatile("s_cmp_gt_u32 %2, 0\n\ts_cbranch_scc1 1f\n\t"
               "s_waitcnt vmcnt(0)\n"
               "1:\n\ts_waitcnt vmcnt(8)"
               : "+v"(qtmp), "+v"(tag) : "s"(n) : "scc");
#pragma unroll
  for (int e = 0; e < 4; ++e) {
    const unsigned t = (tag[e] & TAG_MASK) + 4u;
    tag[e] = t >= 4u * (e == 0 ? 128 : e == 1 ? 127 : e == 2 ? 125 : 123) ? 0u : t;
  }
  asm volatile("" : "+v"(tag));
  reinterpret_cast<u32x4*>(lds + LDS_Q)[tid] = qtmp;
  if (tid == 0) reinterpret_cast<int*>(lds)[LDS_CNT] = 0;
  wg_barrier();
  if (1 < n) issue_k(1);
  if (2 < n) issue_k(2);
  u32x4 qf[4];
#pragma unroll
  for (int f = 0; f < 4; ++f) qf[f] = reinterpret_cast<const u32x4*>(lds + LDS_Q)[(4 * hf + f) * 64 + lane];

  constexpr float C2 = 0.088388347648318440f * 1.4426950408889634f;
  float sc[NU][4];
#pragma unroll
  for (int i = 0; i < NU; ++i) {
    if (i < n) {
      const int tok0 = (gp + i * 1024) * 16;
      wait_groups(i == 0 ? min(n, 3) - 1 : i == 3 ? 2 : n - 1, buf[i]);
      f32x4 d[4];
#pragma unroll
      for (int e = 0; e < 4; ++e) {
        d[e] = mfma32(buf[i][2 * e], qf[2 * (e & 1)], f32x4{0.f, 0.f, 0.f, 0.f});
        d[e] = mfma32(buf[i][2 * e + 1], qf[2 * (e & 1) + 1], d[e]);
      }
      __builtin_amdgcn_sched_barrier(0);
      if (i == 0) { if (3 < n) issue_k(3); __builtin_amdgcn_s_setprio(0); issue_v(0); }
      if (i == 1) issue_v(1);
#pragma unroll
      for (int o = 0; o < 2; ++o)
#pragma unroll
        for (int a = 0; a < 2; ++a) {
          const float u = lo ? d[2 * o][2 * a] : d[2 * o + 1][2 * a];
          const float w = lo ? d[2 * o + 1][2 * a + 1] : d[2 * o][2 * a + 1];
          const float s = (u + dpp<ROR8>(w)) * C2;
          sc[i][2 * o + a] = (tok0 + 8 * o + 2 * g + a < len) ? s : -INFINITY;
        }
    } else {
#pragma unroll
      for (int k = 0; k < 4; ++k) sc[i][k] = -INFINITY;
    }
  }
  u32x2 pf[NU - 1];
  float mx = -INFINITY, pl[4];
  {
#pragma unroll
    for (int i = 0; i < NU; ++i) mx = fmaxf(mx, fmaxf(fmaxf(sc[i][0], sc[i][1]), fmaxf(sc[i][2], sc[i][3])));
    mx = xg_max(mx);
    const float ms = (mx == -INFINITY) ? 0.f : mx;
    float ls = 0.f;
#pragma unroll
    for (int i = 0; i < NU; ++i) {
#pragma unroll
      for (int k = 0; k < 4; ++k) { sc[i][k] = __builtin_amdgcn_exp2f(sc[i][k] - ms); ls += sc[i][k]; }
      if (i < NU - 1) pf[i] = u32x2{pk_bf16(sc[i][0], sc[i][1]), pk_bf16(sc[i][2], sc[i][3])};
    }
#pragma unroll
    for (int k = 0; k < 4; ++k) pl[k] = n > 3 ? sc[3][k] : n > 2 ? sc[2][k] : n > 1 ? sc[1][k] : sc[0][k];
    ls = xg_sum(ls);
    if (g == 0) { lds[LDS_ML + c * 16 + wave] = mx; lds[LDS_ML + c * 16 + 8 + wave] = ls; }
    asm volatile("s_waitcnt lgkmcnt(0)\n\t"
                 "s_mov_b64 exec, 1\n\t"
                 "ds_add_u32 %0, %1\n\t"
                 "s_mov_b64 exec, -1"
                 :: "v"((int)(uintptr_t)(__attribute__((address_space(3))) float*)(lds + LDS_CNT)), "v"(1) : "memory");
  }
  f32x4 acc[8];
#pragma unroll
  for (int k = 0; k < 8; ++k) acc[k] = f32x4{0.f, 0.f, 0.f, 0.f};
  auto pv = [&](u32x4 (&vb)[8], u32x2 p) {
    const u32x4 b = {lo ? p[0] : 0u, lo ? p[1] : 0u, lo ? 0u : p[0], lo ? 0u : p[1]};
#pragma unroll
    for (int k = 0; k < 8; ++k) {
      const unsigned sel = (k & 1) ? 0x07060302u : 0x05040100u;
      const u32x4 a = {__builtin_amdgcn_perm(vb[1][k >> 1], vb[0][k >> 1], sel), __builtin_amdgcn_perm(vb[5][k >> 1], vb[4][k >> 1], sel),
                       __builtin_amdgcn_perm(vb[3][k >> 1], vb[2][k >> 1], sel), __builtin_amdgcn_perm(vb[7][k >> 1], vb[6][k >> 1], sel)};
      acc[k] = mfma32(a, b, acc[k]);
    }
  };
#pragma unroll
  for (int i = 0; i < NU - 1; ++i) {
    if (i + 1 < n) {
      wait_v<8>(buf[i]);
      if (i + 2 < NU && i + 2 < n) issue_v(i + 2);
      pv(buf[i], pf[i]);
    }
  }
  u32x4 pc;
  asm volatile("global_load_dwordx4 %0, %1, %2 sc1" : "=&v"(pc) : "v"(tid * 16), "s"(inbox));
  const int h = tid >> 5, d4 = tid & 31;
  unsigned low[4];
  u32x2 plast;
  {
    for (unsigned spin = 0; spin < (1u << 20); ++spin) {
      if (__builtin_amdgcn_readfirstlane(__hip_atomic_load(reinterpret_cast<int*>(lds) + LDS_CNT, __ATOMIC_RELAXED, __HIP_MEMORY_SCOPE_WORKGROUP)) >= NWAVE) break;
      __builtin_amdgcn_s_sleep(2);
    }
    asm volatile("" ::: "memory");
    float f = 0.f;
#pragma unroll
    for (int r = 0; r < 2; ++r) {
      const float* ml = lds + LDS_ML + (r ? h : c) * 16;
      const f32x4 m0 = *reinterpret_cast<const f32x4*>(ml), m1 = *reinterpret_cast<const f32x4*>(ml + 4);
      const f32x4 l0 = *reinterpret_cast<const f32x4*>(ml + 8), l1 = *reinterpret_cast<const f32x4*>(ml + 12);
      const float M = fmaxf(fmaxf(fmaxf(m0[0], m0[1]), fmaxf(m0[2], m0[3])), fmaxf(fmaxf(m1[0], m1[1]), fmaxf(m1[2], m1[3])));
      const float Ms = (M == -INFINITY) ? 0.f : M;
      float L = 0.f;
#pragma unroll
      for (int w = 0; w < 4; ++w) L += __builtin_amdgcn_exp2f(m0[w] - Ms) * l0[w];
#pragma unroll
      for (int w = 0; w < 4; ++w) L += __builtin_amdgcn_exp2f(m1[w] - Ms) * l1[w];
      if (r == 0) {
        f = L > 0.f ? __builtin_amdgcn_exp2f(mx - Ms) * __builtin_amdgcn_rcpf(L) : 0.f;
      } else {
        const unsigned lse = __float_as_uint(M + __builtin_amdgcn_logf(L));
#pragma unroll
        for (int e = 0; e < 4; ++e) low[e] = tag[e] | ((lse >> (8 * (d4 & 3) + 2 * e)) & 3u);
      }
    }
#pragma unroll
    for (int k = 0; k < 8; ++k) acc[k] *= f;
    plast = u32x2{pk_bf16(pl[0] * f, pl[1] * f), pk_bf16(pl[2] * f, pl[3] * f)};
    asm volatile("" : "+v"(plast), "+v"(low[0]), "+v"(low[1]), "+v"(low[2]), "+v"(low[3]));
  }
#pragma unroll
  for (int i = 0; i < NU; ++i) {
    if (i + 1 == n) {
      wait_v<1>(buf[i]);
      pv(buf[i], plast);
    }
  }
  __builtin_amdgcn_sched_barrier(0);
  asm volatile("s_waitcnt vmcnt(1)" ::: "memory");
  __builtin_amdgcn_sched_barrier(0);
  {
    float* rec = lds + (wave * 16 + c) * D;
#pragma unroll
    for (int k = 0; k < 8; ++k) *reinterpret_cast<f32x4*>(rec + 32 * g + 4 * (k ^ (c & 7))) = acc[k];
  }
  const int moff = ((16 * hf + h) * 8 + (d4 >> 2)) * INBOX_BYTES + wi * 64 + (d4 & 3) * 16;
  wg_barrier();
  {
    f32x4 po[NWAVE];
#pragma unroll
    for (int w = 0; w < NWAVE; ++w) po[w] = *reinterpret_cast<const f32x4*>(lds + (w * 16 + h) * D + 4 * (d4 ^ (h & 7)));
    const f32x4 o = ((po[0] + po[1]) + (po[2] + po[3])) + ((po[4] + po[5]) + (po[6] + po[7]));
    u32x4 msg;
#pragma unroll
    for (int e = 0; e < 4; ++e) msg[e] = (__float_as_uint(o[e]) & DATA_MASK) | low[e];
    asm volatile("global_store_dwordx4 %0, %1, %2 sc1\n\ts_nop 1" :: "v"(moff), "v"(msg), "s"(ws) : "memory");
    asm volatile("s_waitcnt vmcnt(1)" : "+v"(pc));
  }
  unsigned long long wmiss = (wg == 128 && wave >= 6) ? ~0ull : 0ull;
  for (unsigned spin = 0; spin < SPIN_CAP; ++spin) {
    unsigned x = 0;
#pragma unroll
    for (int e = 0; e < 4; ++e) x |= pc[e] ^ tag[e];
    x &= TAG_MASK;
    x |= __builtin_amdgcn_mov_dpp(x, QX1, 0xf, 0xf, true);
    x |= __builtin_amdgcn_mov_dpp(x, QX2, 0xf, 0xf, true);
    const unsigned long long miss = __builtin_amdgcn_ballot_w64(x != 0u);
    if ((miss | wmiss) == 0ull) break;
    if (__builtin_popcountll(miss) > 16) __builtin_amdgcn_s_sleep(16);
    if (wmiss == 0ull) {
      asm volatile("s_mov_b64 exec, %2\n\t"
                   "global_load_dwordx4 %0, %1, %3 sc1\n\t"
                   "s_mov_b64 exec, -1\n\t"
                   "s_waitcnt vmcnt(0)"
                   : "+v"(pc) : "v"(tid * 16), "s"(miss), "s"(inbox) : "memory");
    } else {
      u32x4 w;
      asm volatile("global_load_dwordx4 %0, %2, %3 sc1\n\t"
                   "global_load_dwordx4 %1, %4, %5 sc1\n\t"
                   "s_waitcnt vmcnt(0)"
                   : "=&v"(pc), "=&v"(w) : "v"(tid * 16), "s"(inbox), "v"((tid - 384) * 64 + (lane & 3) * 16), "s"(ws) : "memory");
      unsigned y = 0;
#pragma unroll
      for (int e = 0; e < 4; ++e) y |= w[e] ^ tag[e];
      wmiss = __builtin_amdgcn_ballot_w64((y & TAG_MASK) != 0u);
    }
  }
  {
    float* mrg = lds + LDS_Q;
    unsigned b = ((pc[0] & 3u) | ((pc[1] & 3u) << 2) | ((pc[2] & 3u) << 4) | ((pc[3] & 3u) << 6)) << (8 * (lane & 3));
    b |= __builtin_amdgcn_mov_dpp(b, QX1, 0xf, 0xf, true);
    b |= __builtin_amdgcn_mov_dpp(b, QX2, 0xf, 0xf, true);
    const float ls = __uint_as_float(b);
    float M = ls;
    M = fmaxf(M, dpp<ROR4>(M)); M = fmaxf(M, dpp<ROR8>(M)); M = xg_max(M);
    const float Ms = (M == -INFINITY) ? 0.f : M;
    const float w0 = __builtin_amdgcn_exp2f(ls - Ms);
    float v[5];
#pragma unroll
    for (int e = 0; e < 4; ++e) v[e] = w0 * __uint_as_float(pc[e] & DATA_MASK);
    v[4] = w0;
#pragma unroll
    for (int e = 0; e < 5; ++e) { v[e] += dpp<ROR4>(v[e]); v[e] += dpp<ROR8>(v[e]); v[e] = xg_sum(v[e]); }
    if (lane < 4) {
      *reinterpret_cast<f32x4*>(mrg + (wave * 4 + lane) * 8) = f32x4{v[0], v[1], v[2], v[3]};
      *reinterpret_cast<f32x2*>(mrg + (wave * 4 + lane) * 8 + 4) = f32x2{v[4], M};
    }
    wg_barrier();
    if (wave == 0) {
      const int r = lane & 31;
      const f32x4 o = *reinterpret_cast<const f32x4*>(mrg + r * 8);
      const f32x2 wm = *reinterpret_cast<const f32x2*>(mrg + r * 8 + 4);
      float MM = wm[1];
      MM = fmaxf(MM, dpp<ROR4>(MM)); MM = fmaxf(MM, dpp<ROR8>(MM)); MM = x16_max(MM);
      const float wt = __builtin_amdgcn_exp2f(wm[1] - MM);
      float u[5] = {o[0] * wt, o[1] * wt, o[2] * wt, o[3] * wt, wm[0] * wt};
#pragma unroll
      for (int e = 0; e < 5; ++e) { u[e] += dpp<ROR4>(u[e]); u[e] += dpp<ROR8>(u[e]); u[e] = x16_sum(u[e]); }
      asm volatile("" : "+v"(u[0]), "+v"(u[1]), "+v"(u[2]), "+v"(u[3]), "+v"(u[4]));
      const int r4 = (lane >> 2) & 3;
      const float val = (r4 == 0 ? u[0] : r4 == 1 ? u[1] : r4 == 2 ? u[2] : u[3]) * __builtin_amdgcn_rcpf(u[4]);
      const int ooff = ((wg >> 3) * D + 32 * ((wg & 7) >> 1) + 4 * (wg & 1) + 8 * r4 + (lane & 3)) * 2;
      if (lane < 16) asm volatile("global_store_short %0, %1, %2" :: "v"(ooff), "v"(pk_bf16(val, val)), "s"(out) : "memory");
      if (wg == 128 && lane < 4) asm volatile("global_store_dwordx4 %0, %1, %2 sc1\n\ts_nop 1" :: "v"(lane * 16), "v"(tag), "s"(ws + WS_EPOCH) : "memory");
    }
  }
}
}

extern "C" void kernel_launch(void* const* d_in, const int* in_sizes, int n_in, void* d_out, int out_size, void* d_ws,
                              size_t ws_size, hipStream_t stream) {
  attn_decode<<<dim3(NWG), dim3(NTHR), 0, stream>>>((const uint16_t*)d_in[0], (const uint16_t*)d_in[1], (const uint16_t*)d_in[2],
                                                    (const int*)d_in[3], (const int*)d_in[4], (char*)d_ws, (uint16_t*)d_out);
}
```
